```python
import torch

def _softmax(x, axis=-1):
    return torch.softmax(x, dim=axis)
def _gelu(x):
    return torch.nn.functional.gelu(x, approximate="tanh")
def _silu(x):
    return torch.nn.functional.silu(x)
def _sigmoid(x):
    return torch.sigmoid(x)
def _layernorm(x, axis=-1, eps=1e-5):
    m = x.mean(dim=axis, keepdim=True)
    v = x.var(dim=axis, keepdim=True, unbiased=False)
    return (x - m) / torch.sqrt(v + eps)
def _scatter_add(tgt, idx, src, axis=0):
    return tgt.clone().index_add_(axis, idx, src)
def _segment_sum(src, seg, n):
    out = torch.zeros((n,) + src.shape[1:], dtype=src.dtype)
    return out.index_add_(0, seg, src)
def _pad(x, pw):
    flat = tuple(v for pair in reversed(pw) for v in pair)
    return torch.nn.functional.pad(x, flat)

kernel_name = 'gated_mix__gqa_block_cached__gqa_block_cached__decode'

def setup_inputs():

    _w = (lambda a: (lambda t: t.to(torch.bfloat16) if t.is_floating_point() else t)(torch.as_tensor(a)))
    def _A_setup():
        B, Sq, D = 1, 1, 640
        Hq, Hkv, Sk, Dh = 32, 32, 16384, 128
        assert Hq % Hkv == 0, (Hq, Hkv)
        return {
            "x": _w((lambda *s: torch.randn(*s, dtype=torch.bfloat16) * 0.05)(B, Sq, D)),
            "k_cache": _w((lambda *s: torch.randn(*s, dtype=torch.bfloat16) * 3.2)(B, Hkv, Sk, Dh)),
            "v_cache": _w((lambda *s: torch.randn(*s, dtype=torch.bfloat16) * 0.05)(B, Hkv, Sk, Dh)),
            "wq": _w((lambda *s: torch.randn(*s, dtype=torch.bfloat16) * 3.2)(D, Hq * Dh) * (D ** -0.5)),
            "wo": _w((lambda *s: torch.randn(*s, dtype=torch.bfloat16) * 0.05)(Hq * Dh, D) * ((Hq * Dh) ** -0.5)),
        }
    _A_in = _A_setup()

    def _B_setup():
        B, Sq, D = 1, 1, 640
        Hq, Hkv, Sk, Dh = 32, 32, 16384, 128
        assert Hq % Hkv == 0, (Hq, Hkv)
        return {
            "x": _w((lambda *s: torch.randn(*s, dtype=torch.bfloat16) * 0.05)(B, Sq, D)),
            "k_cache": _w((lambda *s: torch.randn(*s, dtype=torch.bfloat16) * 3.2)(B, Hkv, Sk, Dh)),
            "v_cache": _w((lambda *s: torch.randn(*s, dtype=torch.bfloat16) * 0.05)(B, Hkv, Sk, Dh)),
            "wq": _w((lambda *s: torch.randn(*s, dtype=torch.bfloat16) * 3.2)(D, Hq * Dh) * (D ** -0.5)),
            "wo": _w((lambda *s: torch.randn(*s, dtype=torch.bfloat16) * 0.05)(Hq * Dh, D) * ((Hq * Dh) ** -0.5)),
        }
    _B_in = _B_setup()

    return {
        "A_x": _A_in["x"],
        "A_k_cache": _A_in["k_cache"],
        "A_v_cache": _A_in["v_cache"],
        "A_wq": _A_in["wq"],
        "A_wo": _A_in["wo"],
        "B_x": _B_in["x"],
        "B_k_cache": _B_in["k_cache"],
        "B_v_cache": _B_in["v_cache"],
        "B_wq": _B_in["wq"],
        "B_wo": _B_in["wo"],
        "gate_w": _w((lambda *s: torch.randn(*s, dtype=torch.bfloat16) * 0.05)(640) * 16.0),
    }

def reference(A_x, A_k_cache, A_v_cache, A_wq, A_wo, B_x, B_k_cache, B_v_cache, B_wq, B_wo, gate_w):
    def _A_ref(x, k_cache, v_cache, wq, wo):
        B, Sq, D = x.shape
        Hq, Dh = 32, 128
        Hkv = k_cache.shape[1]
        rep = Hq // Hkv
        q = (lambda x, *d: x.permute(*d))((x @ wq).reshape(B, Sq, Hq, Dh), 0, 2, 1, 3)
        k = (lambda x, n, axis: torch.repeat_interleave(x, n, dim=axis))(k_cache, rep, axis=1)
        v = (lambda x, n, axis: torch.repeat_interleave(x, n, dim=axis))(v_cache, rep, axis=1)
        scores = torch.einsum("bhqd,bhkd->bhqk", q, k) * (Dh ** -0.5)
        p = _softmax(scores, axis=-1)
        o = torch.einsum("bhqk,bhkd->bhqd", p, v)
        return (lambda x, *d: x.permute(*d))(o, 0, 2, 1, 3).reshape(B, Sq, Hq * Dh) @ wo

    def _B_ref(x, k_cache, v_cache, wq, wo):
        B, Sq, D = x.shape
        Hq, Dh = 32, 128
        Hkv = k_cache.shape[1]
        rep = Hq // Hkv
        q = (lambda x, *d: x.permute(*d))((x @ wq).reshape(B, Sq, Hq, Dh), 0, 2, 1, 3)
        k = (lambda x, n, axis: torch.repeat_interleave(x, n, dim=axis))(k_cache, rep, axis=1)
        v = (lambda x, n, axis: torch.repeat_interleave(x, n, dim=axis))(v_cache, rep, axis=1)
        scores = torch.einsum("bhqd,bhkd->bhqk", q, k) * (Dh ** -0.5)
        p = _softmax(scores, axis=-1)
        o = torch.einsum("bhqk,bhkd->bhqd", p, v)
        return (lambda x, *d: x.permute(*d))(o, 0, 2, 1, 3).reshape(B, Sq, Hq * Dh) @ wo

    x = A_x
    g = _sigmoid((x * gate_w).sum(-1, keepdims=True))
    a = _A_ref(x, A_k_cache, A_v_cache, A_wq, A_wo)
    b = _B_ref(x, B_k_cache, B_v_cache, B_wq, B_wo)
    return g * a + (1 - g) * b
```

```cpp
#include <hip/hip_runtime.h>
#include <hip/hip_bf16.h>
#include <stdint.h>

#define GAS __attribute__((address_space(1)))
#define LAS __attribute__((address_space(3)))
typedef unsigned v4u __attribute__((ext_vector_type(4)));
typedef unsigned v2u __attribute__((ext_vector_type(2)));
typedef float v4f __attribute__((ext_vector_type(4)));
typedef float v2f __attribute__((ext_vector_type(2)));
typedef __bf16 bf16x2 __attribute__((ext_vector_type(2)));

namespace {
constexpr int DM = 640, NH = 32, DH = 128, SK = 16384;
constexpr int NSPLIT = 4, NWG = 2 * NH * NSPLIT;
constexpr int NCONS = 10, NTHR = 64 * (NCONS + 1);
constexpr int TOK = SK / NSPLIT;
constexpr int UNIT_B = 4096;
constexpr int NUK = TOK / 16, NU = 2 * NUK;
#define RING_U 32
#define WIN 32
#define W_NT 1
constexpr int LAG = (WIN - 4) / 4;
constexpr int MAXK = (NUK + NCONS - 1) / NCONS;
constexpr int NRED = 40;
constexpr float C2 = 0.08838834764831845f * 1.4426950408889634f;
constexpr unsigned SPIN_LDS = 1u << 22, SPIN_GLB = 1u << 18;

constexpr int L_RING = 0;
constexpr int L_SC = RING_U * UNIT_B;
constexpr int L_CTL = L_SC + NCONS * MAXK * 64;
constexpr int CTL_FULL = 0, CTL_BAR = 1, CTL_ABORT = 3, CTL_PROG = 4  , CTL_WORDS = 16;
constexpr int L_X = L_CTL + CTL_WORDS * 4;
constexpr int L_Q = L_X + NCONS * DH * 4;
constexpr int L_RED = L_Q + 512;
constexpr int LDS_BYTES = L_RED + 8 * 16 * 4 + 16;
static_assert(LDS_BYTES <= 160 * 1024, "LDS");
static_assert(L_SC % 16 == 0 && L_CTL % 16 == 0 && L_X % 16 == 0 && L_Q % 16 == 0 && L_RED % 16 == 0, "align");
static_assert(8 * DM * 4 <= RING_U * UNIT_B, "y reduction scratch reuses the ring");
constexpr int WS_EPOCH = 0;
constexpr int WS_ML = 256;
constexpr int WS_Y = WS_ML + 64 * NSPLIT * NCONS * 16;
constexpr int WS_END = WS_Y + NWG * DM * 8;
static_assert(WS_Y % 128 == 0, "line aligned");

struct Params {
  const unsigned short* x; const unsigned short* k[2]; const unsigned short* v[2]; const unsigned short* wq[2]; const unsigned short* wo[2];
  const unsigned short* gate; unsigned short* out; unsigned char* ws;
};

__device__ __forceinline__ float dot2(unsigned a, unsigned b, float c) {
  return __builtin_amdgcn_fdot2_f32_bf16(__builtin_bit_cast(bf16x2, a), __builtin_bit_cast(bf16x2, b), c, false);
}
template <int CTRL> __device__ __forceinline__ float dpp(float x) {
  return __builtin_bit_cast(float, __builtin_amdgcn_mov_dpp(__builtin_bit_cast(int, x), CTRL, 0xf, 0xf, true));
}
__device__ __forceinline__ float row16_sum(float x) {
  x += dpp<0x128>(x); x += dpp<0x124>(x); x += dpp<0x122>(x); x += dpp<0x121>(x); return x;
}
__device__ __forceinline__ float x16_sum(float x) {
  auto s = __builtin_amdgcn_permlane16_swap(__float_as_uint(x), __float_as_uint(x), false, false);
  return __uint_as_float(s[0]) + __uint_as_float(s[1]);
}
__device__ __forceinline__ float x32_sum(float x) {
  auto s = __builtin_amdgcn_permlane32_swap(__float_as_uint(x), __float_as_uint(x), false, false);
  return __uint_as_float(s[0]) + __uint_as_float(s[1]);
}
__device__ __forceinline__ float x16_max(float x) {
  auto s = __builtin_amdgcn_permlane16_swap(__float_as_uint(x), __float_as_uint(x), false, false);
  return fmaxf(__uint_as_float(s[0]), __uint_as_float(s[1]));
}
__device__ __forceinline__ float x32_max(float x) {
  auto s = __builtin_amdgcn_permlane32_swap(__float_as_uint(x), __float_as_uint(x), false, false);
  return fmaxf(__uint_as_float(s[0]), __uint_as_float(s[1]));
}
__device__ __forceinline__ float wave_sum(float x) { return x32_sum(x16_sum(row16_sum(x))); }
__device__ __forceinline__ float wave_max(float x) {
  x = fmaxf(x, dpp<0x128>(x)); x = fmaxf(x, dpp<0x124>(x)); x = fmaxf(x, dpp<0x122>(x)); x = fmaxf(x, dpp<0x121>(x));
  return x32_max(x16_max(x));
}
__device__ __forceinline__ unsigned bf16_hi(float f) {
  unsigned u = __float_as_uint(f);
  u += 0x7fffu + ((u >> 16) & 1u);
  return u & 0xffff0000u;
}
__device__ __forceinline__ float bf16_rnd(float f) { return __uint_as_float(bf16_hi(f)); }
__device__ __forceinline__ v4u ldw(const GAS v4u* p) {
  return __builtin_nontemporal_load(p);
}
__device__ __forceinline__ void dma4(unsigned lds_dst, unsigned voff, const void* src) {
  asm volatile(
      "s_mov_b32 m0, %0\n\ts_nop 0\n\t"
      "global_load_lds_dwordx4 %1, %2 nt\n\t"
      "global_load_lds_dwordx4 %1, %2 offset:1024 nt\n\t"
      "global_load_lds_dwordx4 %1, %2 offset:2048 nt\n\t"
      "global_load_lds_dwordx4 %1, %2 offset:3072 nt"
      :: "s"(lds_dst), "v"(voff), "s"(src) : "memory");
}
template <int N> __device__ __forceinline__ void wait_vm() { asm volatile("s_waitcnt vmcnt(%0)" :: "i"(N) : "memory"); }
__device__ __forceinline__ void wait_lgkm() { asm volatile("s_waitcnt lgkmcnt(0)" ::: "memory"); }

typedef volatile LAS unsigned* ctl_t;
__device__ __forceinline__ void wait_full(ctl_t ctl, unsigned u) {
  unsigned sp = 0;
  while (ctl[CTL_FULL] <= u) { __builtin_amdgcn_s_sleep(1); if (++sp > SPIN_LDS) { ctl[CTL_ABORT] = 1u; break; } }
  asm volatile("" ::: "memory");
}
__device__ __forceinline__ void cbar(ctl_t ctl, unsigned target, int lane) {
  wait_lgkm();
  if (lane == 0) __hip_atomic_fetch_add((LAS unsigned*)(ctl + CTL_BAR), 1u, __ATOMIC_RELAXED, __HIP_MEMORY_SCOPE_WORKGROUP);
  unsigned sp = 0;
  while (ctl[CTL_BAR] < target) { if (++sp > SPIN_LDS) { ctl[CTL_ABORT] = 1u; break; } }
  asm volatile("" ::: "memory");
}

__global__ void __launch_bounds__(NTHR) fused_attn(Params p) {
  __shared__ __attribute__((aligned(16))) unsigned char smem[LDS_BYTES];
  LAS unsigned char* L = (LAS unsigned char*)smem;
  ctl_t ctl = (ctl_t)(L + L_CTL);
  const int tid = threadIdx.x, lane = tid & 63, wave = __builtin_amdgcn_readfirstlane(tid >> 6);
  const int b = blockIdx.x, hh = b & 63, sp = b >> 6, br = hh >> 5, h = hh & 31;
  const size_t kv_off = ((size_t)h * SK + (size_t)sp * TOK) * DH;

  if (wave == 0) {
    __builtin_amdgcn_s_setprio(3);
    const unsigned char* kb = (const unsigned char*)(p.k[br] + kv_off);
    const unsigned char* vb = (const unsigned char*)(p.v[br] + kv_off);
    const unsigned ring = (unsigned)(uintptr_t)(L + L_RING);
    const unsigned voff = lane * 16;
#pragma unroll
    for (int u = 0; u < LAG; ++u) dma4(ring + u * UNIT_B, voff, kb + u * UNIT_B);
    if (lane < CTL_WORDS) ctl[lane] = (lane >= CTL_PROG && lane < CTL_PROG + NCONS) ? (unsigned)(lane - CTL_PROG) : (lane >= CTL_PROG + NCONS ? 0xffffffffu : 0u);
    wait_lgkm();
    __builtin_amdgcn_s_barrier();
    unsigned free_upto = RING_U, landed = 0;
    for (unsigned u = LAG; u < (unsigned)NU; ++u) {
      if (u >= free_upto) {
        wait_vm<0>();
        landed = u;
        ctl[CTL_FULL] = u;
        unsigned spn = 0;
        for (;;) {
          const v4u a = *(volatile LAS v4u*)(ctl + CTL_PROG), c = *(volatile LAS v4u*)(ctl + CTL_PROG + 4), d = *(volatile LAS v4u*)(ctl + CTL_PROG + 8);
          unsigned mn = min(min(min(a.x, a.y), min(a.z, a.w)), min(min(min(c.x, c.y), min(c.z, c.w)), min(min(d.x, d.y), min(d.z, d.w))));
          free_upto = __builtin_amdgcn_readfirstlane(mn) + RING_U;
          if (u < free_upto) break;
          __builtin_amdgcn_s_sleep(1);
          if (++spn > SPIN_LDS) { ctl[CTL_ABORT] = 1u; free_upto = 0xffffffu; break; }
        }
      }
      const unsigned char* src = (u < (unsigned)NUK) ? kb + (size_t)u * UNIT_B : vb + (size_t)(u - NUK) * UNIT_B;
      dma4(ring + (u % RING_U) * UNIT_B, voff, src);
      wait_vm<WIN - 4>();
      landed = max(landed, u + 1 - LAG);
      ctl[CTL_FULL] = landed;
    }
#define DRAIN_STEP(K) if (LAG > (K)) { wait_vm<4 * (K)>(); ctl[CTL_FULL] = NU - (K); }
    DRAIN_STEP(14) DRAIN_STEP(13) DRAIN_STEP(12) DRAIN_STEP(11) DRAIN_STEP(10) DRAIN_STEP(9) DRAIN_STEP(8) DRAIN_STEP(7)
    DRAIN_STEP(6) DRAIN_STEP(5) DRAIN_STEP(4) DRAIN_STEP(3) DRAIN_STEP(2) DRAIN_STEP(1) DRAIN_STEP(0)
#undef DRAIN_STEP
    return;
  }

  const int c = wave - 1, ct = tid - 64;
  const int r = lane >> 4, c16 = lane & 15;
  v4u W[16];
  unsigned ep;
  {
    const int rr = ct >> 4;
    const GAS v4u* xp = (const GAS v4u*)(p.x + 16 * rr);
    const v4u x0 = xp[0], x1 = xp[1];
    ep = __hip_atomic_load((GAS unsigned*)(p.ws + WS_EPOCH), __ATOMIC_RELAXED, __HIP_MEMORY_SCOPE_AGENT);
    const GAS v4u* wq = (const GAS v4u*)(p.wq[br] + (size_t)(16 * rr) * (NH * DH) + h * DH + 8 * c16);
#pragma unroll
    for (int i = 0; i < 16; ++i) W[i] = ldw(wq + (size_t)i * (NH * DH / 8));
    __builtin_amdgcn_sched_barrier(0);
    __builtin_amdgcn_s_barrier();
    __builtin_amdgcn_sched_barrier(0);
    float qa[8];
#pragma unroll
    for (int e = 0; e < 8; ++e) qa[e] = 0.f;
#pragma unroll
    for (int i = 0; i < 16; ++i) {
      const unsigned xd = (i < 8) ? x0[(i >> 1) & 3] : x1[(i >> 1) & 3];
      const unsigned xlo = (i & 1) ? (xd >> 16) : (xd & 0xffffu), xhi = xlo << 16;
#pragma unroll
      for (int j = 0; j < 4; ++j) { qa[2 * j] = dot2(W[i][j], xlo, qa[2 * j]); qa[2 * j + 1] = dot2(W[i][j], xhi, qa[2 * j + 1]); }
    }
#pragma unroll
    for (int e = 0; e < 8; ++e) qa[e] = x32_sum(x16_sum(qa[e]));
    LAS float* part = (LAS float*)(L + L_X);
    if (r == 0) {
      *(LAS v4f*)(part + c * DH + 8 * c16) = v4f{qa[0], qa[1], qa[2], qa[3]};
      *(LAS v4f*)(part + c * DH + 8 * c16 + 4) = v4f{qa[4], qa[5], qa[6], qa[7]};
    }
  }
  const int wa = ct / 80, wb = ct - wa * 80;
  {
    const GAS v4u* wo = (const GAS v4u*)(p.wo[br] + (size_t)(h * DH + wa) * DM + 8 * wb);
#pragma unroll
    for (int i = 0; i < 16; ++i) W[i] = ldw(wo + (size_t)i * (8 * DM / 8));
  }
  cbar(ctl, NCONS, lane);
  if (ct < 64) {
    const LAS float* part = (const LAS float*)(L + L_X);
    float s0 = 0.f, s1 = 0.f;
#pragma unroll
    for (int w = 0; w < NCONS; ++w) { const v2f t = *(const LAS v2f*)(part + w * DH + 2 * ct); s0 += t.x; s1 += t.y; }
    ((LAS unsigned*)(L + L_Q))[ct] = (bf16_hi(s0) >> 16) | bf16_hi(s1);
  }
  cbar(ctl, 2 * NCONS, lane);
  const v4u q = *(const LAS v4u*)(L + L_Q + 16 * c16);
  const unsigned tag = __builtin_amdgcn_readfirstlane(ep) * 1664525u + 1013904223u;

  LAS unsigned char* sc = L + L_SC + c * (MAXK * 64);
  float mrun = -INFINITY;
  int nk = 0;
  for (int u = c; u < NUK; u += NCONS, ++nk) {
    wait_full(ctl, u);
    const LAS unsigned char* base = L + L_RING + (u % RING_U) * UNIT_B + lane * 16;
    v4u kk[4];
#pragma unroll
    for (int i = 0; i < 4; ++i) kk[i] = *(const LAS v4u*)(base + i * 1024);
    wait_lgkm();
    asm volatile("" : "+v"(kk[0]), "+v"(kk[1]), "+v"(kk[2]), "+v"(kk[3]));
    ctl[CTL_PROG + c] = (u + NCONS < NUK) ? (unsigned)(u + NCONS) : (unsigned)(NUK + c);
    float s[4];
#pragma unroll
    for (int i = 0; i < 4; ++i) {
      float d = dot2(kk[i][0], q[0], 0.f); d = dot2(kk[i][1], q[1], d); d = dot2(kk[i][2], q[2], d); d = dot2(kk[i][3], q[3], d);
      s[i] = row16_sum(d);
    }
    mrun = fmaxf(fmaxf(mrun, fmaxf(s[0], s[1])), fmaxf(s[2], s[3]));
    if (c16 == 0) *(LAS v4f*)(sc + nk * 64 + r * 16) = v4f{s[0], s[1], s[2], s[3]};
  }
  const float mw = x32_max(x16_max(mrun));
  const __amdgpu_buffer_rsrc_t wsr = __builtin_amdgcn_make_buffer_rsrc(p.ws, 0, WS_END, 0x00020000);
  {
    float l = 0.f;
    wait_lgkm();
    for (int idx = lane; idx < nk * 16; idx += 64) {
      const float s = ((const LAS float*)sc)[idx];
      const float e = __builtin_amdgcn_exp2f((s - mw) * C2);
      l += e;
      ((LAS unsigned*)sc)[idx] = bf16_hi(e);
    }
    l = wave_sum(l);
    if (lane == 0) {
      const v4u g = {__float_as_uint(mw * C2), tag, __float_as_uint(l), tag};
      __builtin_amdgcn_raw_buffer_store_b128(g, wsr, WS_ML + (hh * (NSPLIT * NCONS) + sp * NCONS + c) * 16, 0, 16);
    }
    wait_lgkm();
  }
  float acc[8];
#pragma unroll
  for (int e = 0; e < 8; ++e) acc[e] = 0.f;
  auto vstep = [&](int v, int k) {
    const int u = NUK + v;
    wait_full(ctl, u);
    const LAS unsigned char* base = L + L_RING + (u % RING_U) * UNIT_B + lane * 16;
    v4u vv[4];
#pragma unroll
    for (int i = 0; i < 4; ++i) vv[i] = *(const LAS v4u*)(base + i * 1024);
    v4u pp = *(const LAS v4u*)(sc + k * 64 + r * 16);
    wait_lgkm();
    asm volatile("" : "+v"(vv[0]), "+v"(vv[1]), "+v"(vv[2]), "+v"(vv[3]), "+v"(pp));
    ctl[CTL_PROG + c] = (unsigned)(u + NCONS);
#pragma unroll
    for (int i = 0; i < 4; ++i) {
      const unsigned phi = pp[i], plo = phi >> 16;
#pragma unroll
      for (int j = 0; j < 4; ++j) { acc[2 * j] = dot2(vv[i][j], plo, acc[2 * j]); acc[2 * j + 1] = dot2(vv[i][j], phi, acc[2 * j + 1]); }
    }
  };
  int v = c, k = 0;
  for (; k < MAXK / 2; v += NCONS, ++k) vstep(v, k);
  const int ml_off = WS_ML + (hh * (NSPLIT * NCONS) + (lane < NSPLIT * NCONS ? lane : 0)) * 16;
  v4u ml = __builtin_amdgcn_raw_buffer_load_b128(wsr, ml_off, 0, 16);
  for (; v < NUK; v += NCONS, ++k) vstep(v, k);
  {
    unsigned spn = 0;
    while (!__all(ml[1] == tag && ml[3] == tag)) {
      __builtin_amdgcn_s_sleep(2);
      ml = __builtin_amdgcn_raw_buffer_load_b128(wsr, ml_off, 0, 16);
      if (++spn > SPIN_GLB) break;
    }
  }
  {
    const float mi = lane < NSPLIT * NCONS ? __uint_as_float(ml[0]) : -INFINITY;
    const float li = lane < NSPLIT * NCONS ? __uint_as_float(ml[2]) : 0.f;
    const float M = wave_max(mi);
    const float Lh = wave_sum(li * __builtin_amdgcn_exp2f(mi - M));
    const float f = __builtin_amdgcn_exp2f(mw * C2 - M) / Lh;
#pragma unroll
    for (int e = 0; e < 8; ++e) acc[e] = x32_sum(x16_sum(acc[e])) * f;
    LAS float* part = (LAS float*)(L + L_X);
    if (r == 0) {
      *(LAS v4f*)(part + c * DH + 8 * c16) = v4f{acc[0], acc[1], acc[2], acc[3]};
      *(LAS v4f*)(part + c * DH + 8 * c16 + 4) = v4f{acc[4], acc[5], acc[6], acc[7]};
    }
  }
  cbar(ctl, 3 * NCONS, lane);
  if (ct < DH) {
    const LAS float* part = (const LAS float*)(L + L_X);
    float s0 = 0.f;
#pragma unroll
    for (int w = 0; w < NCONS; ++w) s0 += part[w * DH + ct];
    ((LAS unsigned*)(L + L_Q))[(ct & 7) * 16 + (ct >> 3)] = bf16_hi(s0);
  }
  cbar(ctl, 4 * NCONS, lane);
  {
    float y[8];
#pragma unroll
    for (int e = 0; e < 8; ++e) y[e] = 0.f;
#pragma unroll
    for (int i4 = 0; i4 < 4; ++i4) {
      const v4u o4 = *(const LAS v4u*)(L + L_Q + wa * 64 + i4 * 16);
#pragma unroll
      for (int ii = 0; ii < 4; ++ii) {
        const unsigned ohi = o4[ii], olo = ohi >> 16;
        const int i = 4 * i4 + ii;
#pragma unroll
        for (int j = 0; j < 4; ++j) { y[2 * j] = dot2(W[i][j], olo, y[2 * j]); y[2 * j + 1] = dot2(W[i][j], ohi, y[2 * j + 1]); }
      }
    }
    LAS float* yr = (LAS float*)(L + L_RING);
    *(LAS v4f*)(yr + wa * DM + 8 * wb) = v4f{y[0], y[1], y[2], y[3]};
    *(LAS v4f*)(yr + wa * DM + 8 * wb + 4) = v4f{y[4], y[5], y[6], y[7]};
  }
  cbar(ctl, 5 * NCONS, lane);
  {
    const LAS float* yr = (const LAS float*)(L + L_RING);
    float s0 = 0.f;
#pragma unroll
    for (int a = 0; a < 8; ++a) s0 += yr[a * DM + ct];
    const unsigned long long g = ((unsigned long long)tag << 32) | __float_as_uint(s0);
    __hip_atomic_store((GAS unsigned long long*)(p.ws + WS_Y) + (size_t)(hh * NSPLIT + sp) * DM + ct, g, __ATOMIC_RELAXED, __HIP_MEMORY_SCOPE_AGENT);
  }
  if (b >= NRED) return;

  LAS float* red = (LAS float*)(L + L_RED);
  if (c == 8) {
    float z = 0.f;
#pragma unroll
    for (int i = 0; i < 10; ++i) {
      const int idx = lane + 64 * i;
      z += bf16_rnd(__uint_as_float((unsigned)p.x[idx] << 16) * __uint_as_float((unsigned)p.gate[idx] << 16));
    }
    z = bf16_rnd(wave_sum(z));
    const float g = bf16_rnd(1.f / (1.f + __expf(-z)));
    if (lane == 0) red[128] = g;
  }
  if (c < 8) {
    const int j = lane & 7, gi = lane >> 3;
    const int off0 = WS_Y + ((c * 32 + gi) * DM + b * 16 + 2 * j) * 8;
    v4u g0, g1, g2, g3;
    bool ok = false;
    unsigned spn = 0;
    for (;;) {
      if (!ok) {
        g0 = __builtin_amdgcn_raw_buffer_load_b128(wsr, off0, 0, 16);
        g1 = __builtin_amdgcn_raw_buffer_load_b128(wsr, off0 + 8 * DM * 8, 0, 16);
        g2 = __builtin_amdgcn_raw_buffer_load_b128(wsr, off0 + 16 * DM * 8, 0, 16);
        g3 = __builtin_amdgcn_raw_buffer_load_b128(wsr, off0 + 24 * DM * 8, 0, 16);
        ok = g0[1] == tag && g0[3] == tag && g1[1] == tag && g1[3] == tag && g2[1] == tag && g2[3] == tag && g3[1] == tag && g3[3] == tag;
      }
      if (__all(ok)) break;
      __builtin_amdgcn_s_sleep(4);
      if (++spn > SPIN_GLB) break;
    }
    float s0 = (__uint_as_float(g0[0]) + __uint_as_float(g1[0])) + (__uint_as_float(g2[0]) + __uint_as_float(g3[0]));
    float s1 = (__uint_as_float(g0[2]) + __uint_as_float(g1[2])) + (__uint_as_float(g2[2]) + __uint_as_float(g3[2]));
    s0 += dpp<0x128>(s0); s1 += dpp<0x128>(s1);
    s0 = x32_sum(x16_sum(s0)); s1 = x32_sum(x16_sum(s1));
    if (lane < 8) *(LAS v2f*)(red + c * 16 + 2 * j) = v2f{s0, s1};
  }
  cbar(ctl, 6 * NCONS, lane);
  if (ct < 8) {
    const float g = red[128], g1 = bf16_rnd(1.f - g);
    unsigned o2[2];
#pragma unroll
    for (int e = 0; e < 2; ++e) {
      const int col = 2 * ct + e;
      const float a = bf16_rnd((red[col] + red[16 + col]) + (red[32 + col] + red[48 + col]));
      const float bb = bf16_rnd((red[64 + col] + red[80 + col]) + (red[96 + col] + red[112 + col]));
      o2[e] = bf16_hi(bf16_rnd(g * a) + bf16_rnd(g1 * bb));
    }
    ((unsigned*)p.out)[b * 8 + ct] = (o2[0] >> 16) | o2[1];
    if (b == 0 && ct == 0) __hip_atomic_store((GAS unsigned*)(p.ws + WS_EPOCH), tag, __ATOMIC_RELAXED, __HIP_MEMORY_SCOPE_AGENT);
  }
}
}

extern "C" void kernel_launch(void* const* d_in, const int* in_sizes, int n_in, void* d_out, int out_size, void* d_ws, size_t ws_size,
                              hipStream_t stream) {
  Params p;
  p.x = (const unsigned short*)d_in[0];
  p.k[0] = (const unsigned short*)d_in[1]; p.v[0] = (const unsigned short*)d_in[2];
  p.wq[0] = (const unsigned short*)d_in[3]; p.wo[0] = (const unsigned short*)d_in[4];
  p.k[1] = (const unsigned short*)d_in[6]; p.v[1] = (const unsigned short*)d_in[7];
  p.wq[1] = (const unsigned short*)d_in[8]; p.wo[1] = (const unsigned short*)d_in[9];
  p.gate = (const unsigned short*)d_in[10];
  p.out = (unsigned short*)d_out;
  p.ws = (unsigned char*)d_ws;
  hipLaunchKernelGGL(fused_attn, dim3(NWG), dim3(NTHR), 0, stream, p);
}
```

```cpp
#include <hip/hip_runtime.h>
#include <hip/hip_bf16.h>
#include <stdint.h>

#define GAS __attribute__((address_space(1)))
#define LAS __attribute__((address_space(3)))
typedef unsigned v4u __attribute__((ext_vector_type(4)));
typedef unsigned v2u __attribute__((ext_vector_type(2)));
typedef float v4f __attribute__((ext_vector_type(4)));
typedef float v2f __attribute__((ext_vector_type(2)));
typedef __bf16 bf16x2 __attribute__((ext_vector_type(2)));

namespace {
constexpr int DM = 640, NH = 32, DH = 128, SK = 16384;
constexpr int NSPLIT = 4, NWG = 2 * NH * NSPLIT;
constexpr int NCONS = 10, NTHR = 64 * (NCONS + 1);
constexpr int TOK = SK / NSPLIT;
constexpr int UNIT_B = 4096;
constexpr int NUK = TOK / 16, NU = 2 * NUK;
#define RING_U 32
#define WIN 32
#define WQ_NT 1
#define WO_NT 1
constexpr int LAG = (WIN - 4) / 4;
constexpr int MAXK = (NUK + NCONS - 1) / NCONS;
constexpr int NRED = 40;
constexpr float C2 = 0.08838834764831845f * 1.4426950408889634f;
constexpr unsigned SPIN_LDS = 1u << 22, SPIN_GLB = 1u << 18;

constexpr int L_RING = 0;
constexpr int L_SC = RING_U * UNIT_B;
constexpr int L_CTL = L_SC + NCONS * MAXK * 64;
constexpr int CTL_FULL = 0, CTL_BAR = 1, CTL_ABORT = 3, CTL_PROG = 4  , CTL_WORDS = 16;
constexpr int L_X = L_CTL + CTL_WORDS * 4;
constexpr int L_Q = L_X + NCONS * DH * 4;
constexpr int L_RED = L_Q + 512;
constexpr int LDS_BYTES = L_RED + 8 * 16 * 4 + 16;
static_assert(LDS_BYTES <= 160 * 1024, "LDS");
static_assert(L_SC % 16 == 0 && L_CTL % 16 == 0 && L_X % 16 == 0 && L_Q % 16 == 0 && L_RED % 16 == 0, "align");
static_assert(8 * DM * 4 <= RING_U * UNIT_B, "y reduction scratch reuses the ring");
constexpr int WS_EPOCH = 0;
constexpr int WS_ML = 256;
constexpr int WS_Y = WS_ML + 64 * NSPLIT * NCONS * 16;
constexpr int WS_END = WS_Y + NWG * DM * 8;
static_assert(WS_Y % 128 == 0, "line aligned");

#define STAMP(i) do {} while (0)
#define STAMPV(i, v) do {} while (0)
struct Params {
  const unsigned short* x; const unsigned short* k[2]; const unsigned short* v[2]; const unsigned short* wq[2]; const unsigned short* wo[2];
  const unsigned short* gate; unsigned short* out; unsigned char* ws;
};

__device__ __forceinline__ float dot2(unsigned a, unsigned b, float c) {
  return __builtin_amdgcn_fdot2_f32_bf16(__builtin_bit_cast(bf16x2, a), __builtin_bit_cast(bf16x2, b), c, false);
}
template <int CTRL> __device__ __forceinline__ float dpp(float x) {
  return __builtin_bit_cast(float, __builtin_amdgcn_mov_dpp(__builtin_bit_cast(int, x), CTRL, 0xf, 0xf, true));
}
__device__ __forceinline__ float row16_sum(float x) {
  x += dpp<0x128>(x); x += dpp<0x124>(x); x += dpp<0x122>(x); x += dpp<0x121>(x); return x;
}
__device__ __forceinline__ float x16_sum(float x) {
  auto s = __builtin_amdgcn_permlane16_swap(__float_as_uint(x), __float_as_uint(x), false, false);
  return __uint_as_float(s[0]) + __uint_as_float(s[1]);
}
__device__ __forceinline__ float x32_sum(float x) {
  auto s = __builtin_amdgcn_permlane32_swap(__float_as_uint(x), __float_as_uint(x), false, false);
  return __uint_as_float(s[0]) + __uint_as_float(s[1]);
}
__device__ __forceinline__ float x16_max(float x) {
  auto s = __builtin_amdgcn_permlane16_swap(__float_as_uint(x), __float_as_uint(x), false, false);
  return fmaxf(__uint_as_float(s[0]), __uint_as_float(s[1]));
}
__device__ __forceinline__ float x32_max(float x) {
  auto s = __builtin_amdgcn_permlane32_swap(__float_as_uint(x), __float_as_uint(x), false, false);
  return fmaxf(__uint_as_float(s[0]), __uint_as_float(s[1]));
}
__device__ __forceinline__ float wave_sum(float x) { return x32_sum(x16_sum(row16_sum(x))); }
__device__ __forceinline__ float wave_max(float x) {
  x = fmaxf(x, dpp<0x128>(x)); x = fmaxf(x, dpp<0x124>(x)); x = fmaxf(x, dpp<0x122>(x)); x = fmaxf(x, dpp<0x121>(x));
  return x32_max(x16_max(x));
}
__device__ __forceinline__ unsigned bf16_hi(float f) {
  unsigned u = __float_as_uint(f);
  u += 0x7fffu + ((u >> 16) & 1u);
  return u & 0xffff0000u;
}
__device__ __forceinline__ float bf16_rnd(float f) { return __uint_as_float(bf16_hi(f)); }
template <int NT> __device__ __forceinline__ v4u ldw(const GAS v4u* p) {
  if constexpr (NT) return __builtin_nontemporal_load(p); else return *p;
}
__device__ __forceinline__ void dma4(unsigned lds_dst, unsigned voff, const void* src) {
  asm volatile(
      "s_mov_b32 m0, %0\n\ts_nop 0\n\t"
      "global_load_lds_dwordx4 %1, %2 nt\n\t"
      "global_load_lds_dwordx4 %1, %2 offset:1024 nt\n\t"
      "global_load_lds_dwordx4 %1, %2 offset:2048 nt\n\t"
      "global_load_lds_dwordx4 %1, %2 offset:3072 nt"
      :: "s"(lds_dst), "v"(voff), "s"(src) : "memory");
}
template <int N> __device__ __forceinline__ void wait_vm() { asm volatile("s_waitcnt vmcnt(%0)" :: "i"(N) : "memory"); }
__device__ __forceinline__ void wait_lgkm() { asm volatile("s_waitcnt lgkmcnt(0)" ::: "memory"); }

typedef volatile LAS unsigned* ctl_t;
__device__ __forceinline__ void wait_full(ctl_t ctl, unsigned u) {
  unsigned sp = 0;
  while (ctl[CTL_FULL] <= u) { __builtin_amdgcn_s_sleep(1); if (++sp > SPIN_LDS) { ctl[CTL_ABORT] = 1u; break; } }
  asm volatile("" ::: "memory");
}
__device__ __forceinline__ unsigned min_prog(ctl_t ctl) {
  const v4u a = *(volatile LAS v4u*)(ctl + CTL_PROG), c = *(volatile LAS v4u*)(ctl + CTL_PROG + 4), d = *(volatile LAS v4u*)(ctl + CTL_PROG + 8);
  const unsigned mn = min(min(min(a.x, a.y), min(a.z, a.w)), min(min(min(c.x, c.y), min(c.z, c.w)), min(min(d.x, d.y), min(d.z, d.w))));
  return __builtin_amdgcn_readfirstlane(mn);
}
__device__ __forceinline__ void cbar(ctl_t ctl, unsigned target, int lane) {
  wait_lgkm();
  if (lane == 0) __hip_atomic_fetch_add((LAS unsigned*)(ctl + CTL_BAR), 1u, __ATOMIC_RELAXED, __HIP_MEMORY_SCOPE_WORKGROUP);
  unsigned sp = 0;
  while (ctl[CTL_BAR] < target) { if (++sp > SPIN_LDS) { ctl[CTL_ABORT] = 1u; break; } }
  asm volatile("" ::: "memory");
}

__global__ void __launch_bounds__(NTHR) fused_attn(Params p) {
  __shared__ __attribute__((aligned(16))) unsigned char smem[LDS_BYTES];
  LAS unsigned char* L = (LAS unsigned char*)smem;
  ctl_t ctl = (ctl_t)(L + L_CTL);
  const int tid = threadIdx.x, lane = tid & 63, wave = __builtin_amdgcn_readfirstlane(tid >> 6);
  const int b = blockIdx.x, hh = b & 63, sp = b >> 6, br = hh >> 5, h = hh & 31;
  const size_t kv_off = ((size_t)h * SK + (size_t)sp * TOK) * DH;

  if (wave == 0) {
    __builtin_amdgcn_s_setprio(3);
    const unsigned char* kb = (const unsigned char*)(p.k[br] + kv_off);
    const unsigned char* vb = (const unsigned char*)(p.v[br] + kv_off);
    const unsigned ring = (unsigned)(uintptr_t)(L + L_RING);
    const unsigned voff = lane * 16;
#pragma unroll
    for (int u = 0; u < LAG; ++u) dma4(ring + u * UNIT_B, voff, kb + u * UNIT_B);
    if (lane < CTL_WORDS) ctl[lane] = (lane >= CTL_PROG && lane < CTL_PROG + NCONS) ? (unsigned)(lane - CTL_PROG) : (lane >= CTL_PROG + NCONS ? 0xffffffffu : 0u);
    wait_lgkm();
    __builtin_amdgcn_s_barrier();
    unsigned free_upto = RING_U, landed = 0;
    for (unsigned u = LAG; u < (unsigned)NU; ++u) {
      if (u + 4 >= free_upto) {
        free_upto = min_prog(ctl) + RING_U;
        if (u >= free_upto) {
          wait_vm<0>();
          landed = u;
          ctl[CTL_FULL] = u;
          unsigned spn = 0;
          while (u >= free_upto) {
            __builtin_amdgcn_s_sleep(1);
            free_upto = min_prog(ctl) + RING_U;
            if (++spn > SPIN_LDS) { ctl[CTL_ABORT] = 1u; free_upto = 0xffffffu; }
          }
        }
      }
      const unsigned char* src = (u < (unsigned)NUK) ? kb + (size_t)u * UNIT_B : vb + (size_t)(u - NUK) * UNIT_B;
      dma4(ring + (u % RING_U) * UNIT_B, voff, src);
      wait_vm<WIN - 4>();
      landed = max(landed, u + 1 - LAG);
      ctl[CTL_FULL] = landed;
    }
#define DRAIN_STEP(K) if (LAG > (K)) { wait_vm<4 * (K)>(); ctl[CTL_FULL] = NU - (K); }
    DRAIN_STEP(14) DRAIN_STEP(13) DRAIN_STEP(12) DRAIN_STEP(11) DRAIN_STEP(10) DRAIN_STEP(9) DRAIN_STEP(8) DRAIN_STEP(7)
    DRAIN_STEP(6) DRAIN_STEP(5) DRAIN_STEP(4) DRAIN_STEP(3) DRAIN_STEP(2) DRAIN_STEP(1) DRAIN_STEP(0)
#undef DRAIN_STEP
    return;
  }

  const int c = wave - 1, ct = tid - 64;
  const int r = lane >> 4, c16 = lane & 15;
  v4u W[16];
  unsigned ep;
  STAMP(0);
  {
    const int rr = ct >> 4;
    __builtin_amdgcn_s_barrier();
    __builtin_amdgcn_sched_barrier(0);
    const GAS v4u* xp = (const GAS v4u*)(p.x + 16 * rr);
    const v4u x0 = xp[0], x1 = xp[1];
    ep = __hip_atomic_load((GAS unsigned*)(p.ws + WS_EPOCH), __ATOMIC_RELAXED, __HIP_MEMORY_SCOPE_AGENT);
    const GAS v4u* wq = (const GAS v4u*)(p.wq[br] + (size_t)(16 * rr) * (NH * DH) + h * DH + 8 * c16);
#pragma unroll
    for (int i = 0; i < 16; ++i) W[i] = ldw<WQ_NT>(wq + (size_t)i * (NH * DH / 8));
    float qa[8];
#pragma unroll
    for (int e = 0; e < 8; ++e) qa[e] = 0.f;
#pragma unroll
    for (int i = 0; i < 16; ++i) {
      const unsigned xd = (i < 8) ? x0[(i >> 1) & 3] : x1[(i >> 1) & 3];
      const unsigned xlo = (i & 1) ? (xd >> 16) : (xd & 0xffffu), xhi = xlo << 16;
#pragma unroll
      for (int j = 0; j < 4; ++j) { qa[2 * j] = dot2(W[i][j], xlo, qa[2 * j]); qa[2 * j + 1] = dot2(W[i][j], xhi, qa[2 * j + 1]); }
    }
#pragma unroll
    for (int e = 0; e < 8; ++e) qa[e] = x32_sum(x16_sum(qa[e]));
    LAS float* part = (LAS float*)(L + L_X);
    if (r == 0) {
      *(LAS v4f*)(part + c * DH + 8 * c16) = v4f{qa[0], qa[1], qa[2], qa[3]};
      *(LAS v4f*)(part + c * DH + 8 * c16 + 4) = v4f{qa[4], qa[5], qa[6], qa[7]};
    }
  }
  STAMP(1);
  cbar(ctl, NCONS, lane);
  if (ct < 64) {
    const LAS float* part = (const LAS float*)(L + L_X);
    float s0 = 0.f, s1 = 0.f;
#pragma unroll
    for (int w = 0; w < NCONS; ++w) { const v2f t = *(const LAS v2f*)(part + w * DH + 2 * ct); s0 += t.x; s1 += t.y; }
    ((LAS unsigned*)(L + L_Q))[ct] = (bf16_hi(s0) >> 16) | bf16_hi(s1);
  }
  cbar(ctl, 2 * NCONS, lane);
  const v4u q = *(const LAS v4u*)(L + L_Q + 16 * c16);
  const unsigned tag = __builtin_amdgcn_readfirstlane(ep) * 1664525u + 1013904223u;

  STAMP(2);
  LAS unsigned char* sc = L + L_SC + c * (MAXK * 64);
  float mrun = -INFINITY;
  int nk = 0;
  for (int u = c; u < NUK; u += NCONS, ++nk) {
    wait_full(ctl, u);
    const LAS unsigned char* base = L + L_RING + (u % RING_U) * UNIT_B + lane * 16;
    v4u kk[4];
#pragma unroll
    for (int i = 0; i < 4; ++i) kk[i] = *(const LAS v4u*)(base + i * 1024);
    wait_lgkm();
    asm volatile("" : "+v"(kk[0]), "+v"(kk[1]), "+v"(kk[2]), "+v"(kk[3]));
    ctl[CTL_PROG + c] = (u + NCONS < NUK) ? (unsigned)(u + NCONS) : (unsigned)(NUK + c);
    float s[4];
#pragma unroll
    for (int i = 0; i < 4; ++i) {
      float d = dot2(kk[i][0], q[0], 0.f); d = dot2(kk[i][1], q[1], d); d = dot2(kk[i][2], q[2], d); d = dot2(kk[i][3], q[3], d);
      s[i] = row16_sum(d);
    }
    mrun = fmaxf(fmaxf(mrun, fmaxf(s[0], s[1])), fmaxf(s[2], s[3]));
    if (c16 == 0) *(LAS v4f*)(sc + nk * 64 + r * 16) = v4f{s[0], s[1], s[2], s[3]};
  }
  STAMP(3);
  const int wa = ct / 80, wb = ct - wa * 80;
  {
    const GAS v4u* wo = (const GAS v4u*)(p.wo[br] + (size_t)(h * DH + wa) * DM + 8 * wb);
#pragma unroll
    for (int i = 0; i < 16; ++i) W[i] = ldw<WO_NT>(wo + (size_t)i * (8 * DM / 8));
  }
  const float mw = x32_max(x16_max(mrun));
  const __amdgpu_buffer_rsrc_t wsr = __builtin_amdgcn_make_buffer_rsrc(p.ws, 0, WS_END, 0x00020000);
  {
    float l = 0.f;
    wait_lgkm();
    for (int idx = lane; idx < nk * 16; idx += 64) {
      const float s = ((const LAS float*)sc)[idx];
      const float e = __builtin_amdgcn_exp2f((s - mw) * C2);
      l += e;
      ((LAS unsigned*)sc)[idx] = bf16_hi(e);
    }
    l = wave_sum(l);
    if (lane == 0) {
      const v4u g = {__float_as_uint(mw * C2), tag, __float_as_uint(l), tag};
      __builtin_amdgcn_raw_buffer_store_b128(g, wsr, WS_ML + (hh * (NSPLIT * NCONS) + sp * NCONS + c) * 16, 0, 16);
    }
    wait_lgkm();
  }
  STAMP(4);
  float acc[8];
#pragma unroll
  for (int e = 0; e < 8; ++e) acc[e] = 0.f;
  auto vstep = [&](int v, int k) {
    const int u = NUK + v;
    wait_full(ctl, u);
    const LAS unsigned char* base = L + L_RING + (u % RING_U) * UNIT_B + lane * 16;
    v4u vv[4];
#pragma unroll
    for (int i = 0; i < 4; ++i) vv[i] = *(const LAS v4u*)(base + i * 1024);
    v4u pp = *(const LAS v4u*)(sc + k * 64 + r * 16);
    wait_lgkm();
    asm volatile("" : "+v"(vv[0]), "+v"(vv[1]), "+v"(vv[2]), "+v"(vv[3]), "+v"(pp));
    ctl[CTL_PROG + c] = (unsigned)(u + NCONS);
#pragma unroll
    for (int i = 0; i < 4; ++i) {
      const unsigned phi = pp[i], plo = phi >> 16;
#pragma unroll
      for (int j = 0; j < 4; ++j) { acc[2 * j] = dot2(vv[i][j], plo, acc[2 * j]); acc[2 * j + 1] = dot2(vv[i][j], phi, acc[2 * j + 1]); }
    }
  };
  int v = c, k = 0;
  for (; k < MAXK / 2; v += NCONS, ++k) vstep(v, k);
  const int ml_off = WS_ML + (hh * (NSPLIT * NCONS) + (lane < NSPLIT * NCONS ? lane : 0)) * 16;
  v4u ml = __builtin_amdgcn_raw_buffer_load_b128(wsr, ml_off, 0, 16);
  for (; v < NUK; v += NCONS, ++k) vstep(v, k);
  STAMP(5);
  {
    unsigned spn = 0;
    while (!__all(ml[1] == tag && ml[3] == tag)) {
      __builtin_amdgcn_s_sleep(2);
      ml = __builtin_amdgcn_raw_buffer_load_b128(wsr, ml_off, 0, 16);
      if (++spn > SPIN_GLB) break;
    }
  }
  {
    const float mi = lane < NSPLIT * NCONS ? __uint_as_float(ml[0]) : -INFINITY;
    const float li = lane < NSPLIT * NCONS ? __uint_as_float(ml[2]) : 0.f;
    const float M = wave_max(mi);
    const float Lh = wave_sum(li * __builtin_amdgcn_exp2f(mi - M));
    const float f = __builtin_amdgcn_exp2f(mw * C2 - M) / Lh;
#pragma unroll
    for (int e = 0; e < 8; ++e) acc[e] = x32_sum(x16_sum(acc[e])) * f;
    LAS float* part = (LAS float*)(L + L_X);
    if (r == 0) {
      *(LAS v4f*)(part + c * DH + 8 * c16) = v4f{acc[0], acc[1], acc[2], acc[3]};
      *(LAS v4f*)(part + c * DH + 8 * c16 + 4) = v4f{acc[4], acc[5], acc[6], acc[7]};
    }
  }
  STAMP(6);
  cbar(ctl, 3 * NCONS, lane);
  if (ct < DH) {
    const LAS float* part = (const LAS float*)(L + L_X);
    float s0 = 0.f;
#pragma unroll
    for (int w = 0; w < NCONS; ++w) s0 += part[w * DH + ct];
    ((LAS unsigned*)(L + L_Q))[(ct & 7) * 16 + (ct >> 3)] = bf16_hi(s0);
  }
  cbar(ctl, 4 * NCONS, lane);
  {
    float y[8];
#pragma unroll
    for (int e = 0; e < 8; ++e) y[e] = 0.f;
#pragma unroll
    for (int i4 = 0; i4 < 4; ++i4) {
      const v4u o4 = *(const LAS v4u*)(L + L_Q + wa * 64 + i4 * 16);
#pragma unroll
      for (int ii = 0; ii < 4; ++ii) {
        const unsigned ohi = o4[ii], olo = ohi >> 16;
        const int i = 4 * i4 + ii;
#pragma unroll
        for (int j = 0; j < 4; ++j) { y[2 * j] = dot2(W[i][j], olo, y[2 * j]); y[2 * j + 1] = dot2(W[i][j], ohi, y[2 * j + 1]); }
      }
    }
    LAS float* yr = (LAS float*)(L + L_RING);
    *(LAS v4f*)(yr + wa * DM + 8 * wb) = v4f{y[0], y[1], y[2], y[3]};
    *(LAS v4f*)(yr + wa * DM + 8 * wb + 4) = v4f{y[4], y[5], y[6], y[7]};
  }
  STAMP(7);
  cbar(ctl, 5 * NCONS, lane);
  {
    const LAS float* yr = (const LAS float*)(L + L_RING);
    float s0 = 0.f;
#pragma unroll
    for (int a = 0; a < 8; ++a) s0 += yr[a * DM + ct];
    const unsigned long long g = ((unsigned long long)tag << 32) | __float_as_uint(s0);
    __hip_atomic_store((GAS unsigned long long*)(p.ws + WS_Y) + (size_t)(hh * NSPLIT + sp) * DM + ct, g, __ATOMIC_RELAXED, __HIP_MEMORY_SCOPE_AGENT);
  }
  STAMP(8);
  if (b >= NRED) return;

  LAS float* red = (LAS float*)(L + L_RED);
  if (c == 8) {
    float z = 0.f;
#pragma unroll
    for (int i = 0; i < 10; ++i) {
      const int idx = lane + 64 * i;
      z += bf16_rnd(__uint_as_float((unsigned)p.x[idx] << 16) * __uint_as_float((unsigned)p.gate[idx] << 16));
    }
    z = bf16_rnd(wave_sum(z));
    const float g = bf16_rnd(1.f / (1.f + __expf(-z)));
    if (lane == 0) red[128] = g;
  }
  if (c < 8) {
    const int j = lane & 7, gi = lane >> 3;
    const int off0 = WS_Y + ((c * 32 + gi) * DM + b * 16 + 2 * j) * 8;
    v4u g0, g1, g2, g3;
    bool ok = false;
    unsigned spn = 0;
    for (;;) {
      if (!ok) {
        g0 = __builtin_amdgcn_raw_buffer_load_b128(wsr, off0, 0, 16);
        g1 = __builtin_amdgcn_raw_buffer_load_b128(wsr, off0 + 8 * DM * 8, 0, 16);
        g2 = __builtin_amdgcn_raw_buffer_load_b128(wsr, off0 + 16 * DM * 8, 0, 16);
        g3 = __builtin_amdgcn_raw_buffer_load_b128(wsr, off0 + 24 * DM * 8, 0, 16);
        ok = g0[1] == tag && g0[3] == tag && g1[1] == tag && g1[3] == tag && g2[1] == tag && g2[3] == tag && g3[1] == tag && g3[3] == tag;
      }
      if (__all(ok)) break;
      __builtin_amdgcn_s_sleep(4);
      if (++spn > SPIN_GLB) break;
    }
    float s0 = (__uint_as_float(g0[0]) + __uint_as_float(g1[0])) + (__uint_as_float(g2[0]) + __uint_as_float(g3[0]));
    float s1 = (__uint_as_float(g0[2]) + __uint_as_float(g1[2])) + (__uint_as_float(g2[2]) + __uint_as_float(g3[2]));
    s0 += dpp<0x128>(s0); s1 += dpp<0x128>(s1);
    s0 = x32_sum(x16_sum(s0)); s1 = x32_sum(x16_sum(s1));
    if (lane < 8) *(LAS v2f*)(red + c * 16 + 2 * j) = v2f{s0, s1};
  }
  STAMP(9);
  cbar(ctl, 6 * NCONS, lane);
  if (ct < 8) {
    const float g = red[128], g1 = bf16_rnd(1.f - g);
    unsigned o2[2];
#pragma unroll
    for (int e = 0; e < 2; ++e) {
      const int col = 2 * ct + e;
      const float a = bf16_rnd((red[col] + red[16 + col]) + (red[32 + col] + red[48 + col]));
      const float bb = bf16_rnd((red[64 + col] + red[80 + col]) + (red[96 + col] + red[112 + col]));
      o2[e] = bf16_hi(bf16_rnd(g * a) + bf16_rnd(g1 * bb));
    }
    ((unsigned*)p.out)[b * 8 + ct] = (o2[0] >> 16) | o2[1];
    STAMP(10);
    if (b == 0 && ct == 0) __hip_atomic_store((GAS unsigned*)(p.ws + WS_EPOCH), tag, __ATOMIC_RELAXED, __HIP_MEMORY_SCOPE_AGENT);
  }
}
}

extern "C" void kernel_launch(void* const* d_in, const int* in_sizes, int n_in, void* d_out, int out_size, void* d_ws, size_t ws_size,
                              hipStream_t stream) {
  Params p;
  p.x = (const unsigned short*)d_in[0];
  p.k[0] = (const unsigned short*)d_in[1]; p.v[0] = (const unsigned short*)d_in[2];
  p.wq[0] = (const unsigned short*)d_in[3]; p.wo[0] = (const unsigned short*)d_in[4];
  p.k[1] = (const unsigned short*)d_in[6]; p.v[1] = (const unsigned short*)d_in[7];
  p.wq[1] = (const unsigned short*)d_in[8]; p.wo[1] = (const unsigned short*)d_in[9];
  p.gate = (const unsigned short*)d_in[10];
  p.out = (unsigned short*)d_out;
  p.ws = (unsigned char*)d_ws;
  hipLaunchKernelGGL(fused_attn, dim3(NWG), dim3(NTHR), 0, stream, p);
}
```

```cpp
#include <hip/hip_runtime.h>
#include <hip/hip_bf16.h>
#include <stdint.h>

#define GAS __attribute__((address_space(1)))
#define LAS __attribute__((address_space(3)))
typedef unsigned v4u __attribute__((ext_vector_type(4)));
typedef unsigned v2u __attribute__((ext_vector_type(2)));
typedef float v4f __attribute__((ext_vector_type(4)));
typedef float v2f __attribute__((ext_vector_type(2)));
typedef __bf16 bf16x2 __attribute__((ext_vector_type(2)));

namespace {
constexpr int DM = 640, NH = 32, DH = 128, SK = 16384;
constexpr int NSPLIT = 4, NWG = 2 * NH * NSPLIT;
constexpr int NCONS = 10, NTHR = 64 * (NCONS + 1);
constexpr int TOK = SK / NSPLIT;
constexpr int UNIT_B = 4096;
constexpr int NUK = TOK / 16, NU = 2 * NUK;
#define RING_U 32
#define WIN 24
#define WQ_NT 1
#define PRE_UNITS 2
#define WO_GAP 4
#define WO_NT 1
constexpr int LAG = (WIN - 4) / 4;
constexpr int MAXK = (NUK + NCONS - 1) / NCONS;
constexpr int NRED = 40;
constexpr float C2 = 0.08838834764831845f * 1.4426950408889634f;
constexpr unsigned SPIN_LDS = 1u << 22, SPIN_GLB = 1u << 18;

constexpr int L_RING = 0;
constexpr int L_SC = RING_U * UNIT_B;
constexpr int L_CTL = L_SC + NCONS * MAXK * 64;
constexpr int CTL_FULL = 0, CTL_BAR = 1, CTL_ABORT = 3, CTL_PROG = 4  , CTL_WORDS = 16;
constexpr int L_X = L_CTL + CTL_WORDS * 4;
constexpr int L_Q = L_X + NCONS * DH * 4;
constexpr int L_RED = L_Q + 512;
constexpr int LDS_BYTES = L_RED + 8 * 16 * 4 + 16;
static_assert(LDS_BYTES <= 160 * 1024, "LDS");
static_assert(L_SC % 16 == 0 && L_CTL % 16 == 0 && L_X % 16 == 0 && L_Q % 16 == 0 && L_RED % 16 == 0, "align");
static_assert(8 * DM * 4 <= RING_U * UNIT_B, "y reduction scratch reuses the ring");
constexpr int WS_EPOCH = 0;
constexpr int WS_ML = 256;
constexpr int WS_Q = WS_ML + 64 * NSPLIT * NCONS * 16;
constexpr int WS_Y = WS_Q + 64 * NSPLIT * DH * 8;
constexpr int WS_END = WS_Y + NWG * DM * 8;
static_assert(WS_Y % 128 == 0, "line aligned");

#define STAMP(i) do {} while (0)
#define STAMPV(i, v) do {} while (0)
#define STAMP_FLUSH() do {} while (0)
typedef const unsigned short* cptr;
struct Params {
  cptr k0, k1, v0, v1, wq0, wq1, wo0, wo1, x, gate;
  unsigned short* out; unsigned char* ws;
};

__device__ __forceinline__ float dot2(unsigned a, unsigned b, float c) {
  return __builtin_amdgcn_fdot2_f32_bf16(__builtin_bit_cast(bf16x2, a), __builtin_bit_cast(bf16x2, b), c, false);
}
template <int CTRL> __device__ __forceinline__ float dpp(float x) {
  return __builtin_bit_cast(float, __builtin_amdgcn_mov_dpp(__builtin_bit_cast(int, x), CTRL, 0xf, 0xf, true));
}
__device__ __forceinline__ float row16_sum(float x) {
  x += dpp<0x128>(x); x += dpp<0x124>(x); x += dpp<0x122>(x); x += dpp<0x121>(x); return x;
}
__device__ __forceinline__ float x16_sum(float x) {
  auto s = __builtin_amdgcn_permlane16_swap(__float_as_uint(x), __float_as_uint(x), false, false);
  return __uint_as_float(s[0]) + __uint_as_float(s[1]);
}
__device__ __forceinline__ float x32_sum(float x) {
  auto s = __builtin_amdgcn_permlane32_swap(__float_as_uint(x), __float_as_uint(x), false, false);
  return __uint_as_float(s[0]) + __uint_as_float(s[1]);
}
__device__ __forceinline__ float x16_max(float x) {
  auto s = __builtin_amdgcn_permlane16_swap(__float_as_uint(x), __float_as_uint(x), false, false);
  return fmaxf(__uint_as_float(s[0]), __uint_as_float(s[1]));
}
__device__ __forceinline__ float x32_max(float x) {
  auto s = __builtin_amdgcn_permlane32_swap(__float_as_uint(x), __float_as_uint(x), false, false);
  return fmaxf(__uint_as_float(s[0]), __uint_as_float(s[1]));
}
__device__ __forceinline__ float wave_sum(float x) { return x32_sum(x16_sum(row16_sum(x))); }
__device__ __forceinline__ float wave_max(float x) {
  x = fmaxf(x, dpp<0x128>(x)); x = fmaxf(x, dpp<0x124>(x)); x = fmaxf(x, dpp<0x122>(x)); x = fmaxf(x, dpp<0x121>(x));
  return x32_max(x16_max(x));
}
__device__ __forceinline__ unsigned bf16_hi(float f) {
  unsigned u = __float_as_uint(f);
  u += 0x7fffu + ((u >> 16) & 1u);
  return u & 0xffff0000u;
}
__device__ __forceinline__ float bf16_rnd(float f) { return __uint_as_float(bf16_hi(f)); }
template <int NT> __device__ __forceinline__ v4u ldw(const GAS v4u* p) {
  if constexpr (NT) return __builtin_nontemporal_load(p); else return *p;
}
__device__ __forceinline__ void dma4(unsigned lds_dst, unsigned voff, const void* src) {
  asm volatile(
      "s_mov_b32 m0, %0\n\ts_nop 0\n\t"
      "global_load_lds_dwordx4 %1, %2 nt\n\t"
      "global_load_lds_dwordx4 %1, %2 offset:1024 nt\n\t"
      "global_load_lds_dwordx4 %1, %2 offset:2048 nt\n\t"
      "global_load_lds_dwordx4 %1, %2 offset:3072 nt"
      :: "s"(lds_dst), "v"(voff), "s"(src) : "memory");
}
template <int N> __device__ __forceinline__ void wait_vm() { asm volatile("s_waitcnt vmcnt(%0)" :: "i"(N) : "memory"); }
__device__ __forceinline__ void wait_lgkm() { asm volatile("s_waitcnt lgkmcnt(0)" ::: "memory"); }

__device__ __forceinline__ void hbar() { asm volatile("s_waitcnt lgkmcnt(0)\n\ts_barrier" ::: "memory"); }
typedef volatile LAS unsigned* ctl_t;
__device__ __forceinline__ void wait_full(ctl_t ctl, unsigned u) {
  unsigned sp = 0;
  while (ctl[CTL_FULL] <= u) { __builtin_amdgcn_s_sleep(1); if (++sp > SPIN_LDS) { ctl[CTL_ABORT] = 1u; break; } }
  asm volatile("" ::: "memory");
}
__device__ __forceinline__ unsigned min_prog(ctl_t ctl) {
  const v4u a = *(volatile LAS v4u*)(ctl + CTL_PROG), c = *(volatile LAS v4u*)(ctl + CTL_PROG + 4), d = *(volatile LAS v4u*)(ctl + CTL_PROG + 8);
  const unsigned mn = min(min(min(a.x, a.y), min(a.z, a.w)), min(min(min(c.x, c.y), min(c.z, c.w)), min(min(d.x, d.y), min(d.z, d.w))));
  return __builtin_amdgcn_readfirstlane(mn);
}
__device__ __forceinline__ void cbar(ctl_t ctl, unsigned target, int lane) {
  wait_lgkm();
  if (lane == 0) __hip_atomic_fetch_add((LAS unsigned*)(ctl + CTL_BAR), 1u, __ATOMIC_RELAXED, __HIP_MEMORY_SCOPE_WORKGROUP);
  unsigned sp = 0;
  while (ctl[CTL_BAR] < target) { if (++sp > SPIN_LDS) { ctl[CTL_ABORT] = 1u; break; } }
  asm volatile("" ::: "memory");
}

__global__ void __launch_bounds__(NTHR) fused_attn(Params p) {
  __shared__ __attribute__((aligned(16))) unsigned char smem[LDS_BYTES];
  LAS unsigned char* L = (LAS unsigned char*)smem;
  ctl_t ctl = (ctl_t)(L + L_CTL);
  const int tid = threadIdx.x, lane = tid & 63, wave = __builtin_amdgcn_readfirstlane(tid >> 6);
  const int b = blockIdx.x, hh = b & 63, sp = b >> 6, br = hh >> 5, h = hh & 31;
  const size_t kv_off = ((size_t)h * SK + (size_t)sp * TOK) * DH;
  cptr pk, pv, pwq, pwo;
  {
    cptr k0 = p.k0, k1 = p.k1, v0 = p.v0, v1 = p.v1, q0 = p.wq0, q1 = p.wq1, o0 = p.wo0, o1 = p.wo1, x_ = p.x, g_ = p.gate;
    unsigned short* out_ = p.out; unsigned char* ws_ = p.ws;
    asm volatile("" : "+s"(k0), "+s"(k1), "+s"(v0), "+s"(v1), "+s"(q0), "+s"(q1), "+s"(o0), "+s"(o1), "+s"(x_), "+s"(g_), "+s"(out_), "+s"(ws_));
    pk = br ? k1 : k0; pv = br ? v1 : v0; pwq = br ? q1 : q0; pwo = br ? o1 : o0;
    p.x = x_; p.gate = g_; p.out = out_; p.ws = ws_;
  }

  if (wave == 0) {
    __builtin_amdgcn_s_setprio(3);
    const unsigned char* kb = (const unsigned char*)(pk + kv_off);
    const unsigned char* vb = (const unsigned char*)(pv + kv_off);
    const unsigned ring = (unsigned)(uintptr_t)(L + L_RING);
    const unsigned voff = lane * 16;
    if (lane < CTL_WORDS) ctl[lane] = (lane >= CTL_PROG && lane < CTL_PROG + NCONS) ? (unsigned)(lane - CTL_PROG) : (lane >= CTL_PROG + NCONS ? 0xffffffffu : 0u);
#pragma unroll
    for (int u = 0; u < PRE_UNITS; ++u) dma4(ring + u * UNIT_B, voff, kb + u * UNIT_B);
    wait_lgkm();
    __builtin_amdgcn_s_barrier();
#pragma unroll
    for (int u = PRE_UNITS; u < LAG; ++u) dma4(ring + u * UNIT_B, voff, kb + u * UNIT_B);
    unsigned free_upto = RING_U, landed = 0;
    for (unsigned u = LAG; u < (unsigned)NU; ++u) {
      if (u + 4 >= free_upto) {
        free_upto = min_prog(ctl) + RING_U;
        if (u >= free_upto) {
          wait_vm<0>();
          landed = u;
          ctl[CTL_FULL] = u;
          unsigned spn = 0;
          while (u >= free_upto) {
            __builtin_amdgcn_s_sleep(1);
            free_upto = min_prog(ctl) + RING_U;
            if (++spn > SPIN_LDS) { ctl[CTL_ABORT] = 1u; free_upto = 0xffffffu; }
          }
        }
      }
      const unsigned char* src = (u < (unsigned)NUK) ? kb + (size_t)u * UNIT_B : vb + (size_t)(u - NUK) * UNIT_B;
      dma4(ring + (u % RING_U) * UNIT_B, voff, src);
      wait_vm<WIN - 4>();
      landed = max(landed, u + 1 - LAG);
      ctl[CTL_FULL] = landed;
    }
#define DRAIN_STEP(K) if (LAG > (K)) { wait_vm<4 * (K)>(); ctl[CTL_FULL] = NU - (K); }
    DRAIN_STEP(14) DRAIN_STEP(13) DRAIN_STEP(12) DRAIN_STEP(11) DRAIN_STEP(10) DRAIN_STEP(9) DRAIN_STEP(8) DRAIN_STEP(7)
    DRAIN_STEP(6) DRAIN_STEP(5) DRAIN_STEP(4) DRAIN_STEP(3) DRAIN_STEP(2) DRAIN_STEP(1) DRAIN_STEP(0)
#undef DRAIN_STEP
    return;
  }

  const int c = wave - 1, ct = tid - 64;
  const int r = lane >> 4, c16 = lane & 15;
  v4u W[16];
  STAMP(0);
  const __amdgpu_buffer_rsrc_t wsr = __builtin_amdgcn_make_buffer_rsrc(p.ws, 0, WS_END, 0x00020000);
  unsigned tag;
  {
    const int row0 = sp * (DM / NSPLIT) + 4 * (ct >> 4);
    const GAS v4u* wq = (const GAS v4u*)(pwq + (size_t)row0 * (NH * DH) + h * DH + 8 * c16);
    v4u w4[4];
#pragma unroll
    for (int i = 0; i < 4; ++i) w4[i] = ldw<WQ_NT>(wq + (size_t)i * (NH * DH / 8));
    const v2u x2 = *(const GAS v2u*)(p.x + row0);
    const unsigned ep = __hip_atomic_load((GAS unsigned*)(p.ws + WS_EPOCH), __ATOMIC_RELAXED, __HIP_MEMORY_SCOPE_AGENT);
    __builtin_amdgcn_sched_barrier(0);
    __builtin_amdgcn_s_barrier();
    __builtin_amdgcn_sched_barrier(0);
    float qa[8];
#pragma unroll
    for (int e = 0; e < 8; ++e) qa[e] = 0.f;
#pragma unroll
    for (int i = 0; i < 4; ++i) {
      const unsigned xd = x2[i >> 1];
      const unsigned xlo = (i & 1) ? (xd >> 16) : (xd & 0xffffu), xhi = xlo << 16;
#pragma unroll
      for (int j = 0; j < 4; ++j) { qa[2 * j] = dot2(w4[i][j], xlo, qa[2 * j]); qa[2 * j + 1] = dot2(w4[i][j], xhi, qa[2 * j + 1]); }
    }
#pragma unroll
    for (int e = 0; e < 8; ++e) qa[e] = x32_sum(x16_sum(qa[e]));
    LAS float* part = (LAS float*)(L + L_X);
    if (r == 0) {
      *(LAS v4f*)(part + c * DH + 8 * c16) = v4f{qa[0], qa[1], qa[2], qa[3]};
      *(LAS v4f*)(part + c * DH + 8 * c16 + 4) = v4f{qa[4], qa[5], qa[6], qa[7]};
    }
    tag = __builtin_amdgcn_readfirstlane(ep) * 1664525u + 1013904223u;
  }
  STAMP(1);
  cbar(ctl, NCONS, lane);
  if (c == 0) {
    const LAS float* part = (const LAS float*)(L + L_X);
    float s0 = 0.f, s1 = 0.f;
#pragma unroll
    for (int w = 0; w < NCONS; ++w) { const v2f t = *(const LAS v2f*)(part + w * DH + 2 * lane); s0 += t.x; s1 += t.y; }
    const int qoff = WS_Q + (hh * NSPLIT * DH + 2 * lane) * 8;
    __builtin_amdgcn_raw_buffer_store_b128(v4u{__float_as_uint(s0), tag, __float_as_uint(s1), tag}, wsr, qoff + sp * DH * 8, 0, 16);
    v4u g0, g1, g2, g3;
    unsigned spn = 0;
    for (;;) {
      g0 = __builtin_amdgcn_raw_buffer_load_b128(wsr, qoff, 0, 16);
      g1 = __builtin_amdgcn_raw_buffer_load_b128(wsr, qoff + DH * 8, 0, 16);
      g2 = __builtin_amdgcn_raw_buffer_load_b128(wsr, qoff + 2 * DH * 8, 0, 16);
      g3 = __builtin_amdgcn_raw_buffer_load_b128(wsr, qoff + 3 * DH * 8, 0, 16);
      const bool ok = g0[1] == tag && g0[3] == tag && g1[1] == tag && g1[3] == tag && g2[1] == tag && g2[3] == tag && g3[1] == tag && g3[3] == tag;
      if (__all(ok) || ++spn > SPIN_GLB) break;
      __builtin_amdgcn_s_sleep(1);
    }
    s0 = (__uint_as_float(g0[0]) + __uint_as_float(g1[0])) + (__uint_as_float(g2[0]) + __uint_as_float(g3[0]));
    s1 = (__uint_as_float(g0[2]) + __uint_as_float(g1[2])) + (__uint_as_float(g2[2]) + __uint_as_float(g3[2]));
    ((LAS unsigned*)(L + L_Q))[lane] = (bf16_hi(s0) >> 16) | bf16_hi(s1);
  }
  cbar(ctl, 2 * NCONS, lane);
  const v4u q = *(const LAS v4u*)(L + L_Q + 16 * c16);

  STAMP(2);
  const int wa = ct / 80, wb = ct - wa * 80;
  const GAS v4u* wo = (const GAS v4u*)(pwo + (size_t)(h * DH + wa) * DM + 8 * wb);
  LAS unsigned char* sc = L + L_SC + c * (MAXK * 64);
  float mrun = -INFINITY;
  int nk = 0, u = c;
  auto kstep = [&]() {
    wait_full(ctl, u);
    const LAS unsigned char* base = L + L_RING + (u % RING_U) * UNIT_B + lane * 16;
    v4u kk[4];
#pragma unroll
    for (int i = 0; i < 4; ++i) kk[i] = *(const LAS v4u*)(base + i * 1024);
    wait_lgkm();
    asm volatile("" : "+v"(kk[0]), "+v"(kk[1]), "+v"(kk[2]), "+v"(kk[3]));
    ctl[CTL_PROG + c] = (u + NCONS < NUK) ? (unsigned)(u + NCONS) : (unsigned)(NUK + c);
    float s[4];
#pragma unroll
    for (int i = 0; i < 4; ++i) {
      float d = dot2(kk[i][0], q[0], 0.f); d = dot2(kk[i][1], q[1], d); d = dot2(kk[i][2], q[2], d); d = dot2(kk[i][3], q[3], d);
      s[i] = row16_sum(d);
    }
    mrun = fmaxf(fmaxf(mrun, fmaxf(s[0], s[1])), fmaxf(s[2], s[3]));
    if (c16 == 0) *(LAS v4f*)(sc + nk * 64 + r * 16) = v4f{s[0], s[1], s[2], s[3]};
    u += NCONS; ++nk;
  };
#pragma unroll
  for (int seg = 0; seg < 4; ++seg) {
#pragma unroll 1
    for (int j = 0; j < WO_GAP; ++j) kstep();
#pragma unroll
    for (int i = 4 * seg; i < 4 * seg + 4; ++i) W[i] = ldw<WO_NT>(wo + (size_t)i * (8 * DM / 8));
    __builtin_amdgcn_sched_barrier(0);
  }
#pragma unroll 1
  while (u < NUK) kstep();
  STAMP(3);
  const float mw = x32_max(x16_max(mrun));
  {
    float l = 0.f;
    wait_lgkm();
    for (int idx = lane; idx < nk * 16; idx += 64) {
      const float s = ((const LAS float*)sc)[idx];
      const float e = __builtin_amdgcn_exp2f((s - mw) * C2);
      l += e;
      ((LAS unsigned*)sc)[idx] = bf16_hi(e);
    }
    l = wave_sum(l);
    if (lane == 0) {
      const v4u g = {__float_as_uint(mw * C2), tag, __float_as_uint(l), tag};
      __builtin_amdgcn_raw_buffer_store_b128(g, wsr, WS_ML + (hh * (NSPLIT * NCONS) + sp * NCONS + c) * 16, 0, 16);
    }
    wait_lgkm();
  }
  STAMP(4);
  float acc[8];
#pragma unroll
  for (int e = 0; e < 8; ++e) acc[e] = 0.f;
  auto vstep = [&](int v, int k) {
    const int u = NUK + v;
    wait_full(ctl, u);
    const LAS unsigned char* base = L + L_RING + (u % RING_U) * UNIT_B + lane * 16;
    v4u vv[4];
#pragma unroll
    for (int i = 0; i < 4; ++i) vv[i] = *(const LAS v4u*)(base + i * 1024);
    v4u pp = *(const LAS v4u*)(sc + k * 64 + r * 16);
    wait_lgkm();
    asm volatile("" : "+v"(vv[0]), "+v"(vv[1]), "+v"(vv[2]), "+v"(vv[3]), "+v"(pp));
    ctl[CTL_PROG + c] = (unsigned)(u + NCONS);
#pragma unroll
    for (int i = 0; i < 4; ++i) {
      const unsigned phi = pp[i], plo = phi >> 16;
#pragma unroll
      for (int j = 0; j < 4; ++j) { acc[2 * j] = dot2(vv[i][j], plo, acc[2 * j]); acc[2 * j + 1] = dot2(vv[i][j], phi, acc[2 * j + 1]); }
    }
  };
  int v = c, k = 0;
#pragma unroll 1
  for (; k < 13; v += NCONS, ++k) vstep(v, k);
  const int ml_off = WS_ML + (hh * (NSPLIT * NCONS) + (lane < NSPLIT * NCONS ? lane : 0)) * 16;
  v4u ml = __builtin_amdgcn_raw_buffer_load_b128(wsr, ml_off, 0, 16);
  const bool gate_wave = (c == 8) && (b < NRED);
  unsigned gx[5], gg[5];
  if (gate_wave) {
#pragma unroll
    for (int i = 0; i < 5; ++i) { gx[i] = ((const GAS unsigned*)p.x)[lane + 64 * i]; gg[i] = ((const GAS unsigned*)p.gate)[lane + 64 * i]; }
  }
#pragma unroll 1
  for (; k < 19; v += NCONS, ++k) vstep(v, k);
  float f;
  {
    unsigned spn = 0;
    while (!__all(ml[1] == tag && ml[3] == tag)) {
      __builtin_amdgcn_s_sleep(2);
      ml = __builtin_amdgcn_raw_buffer_load_b128(wsr, ml_off, 0, 16);
      if (++spn > SPIN_GLB) break;
    }
    const float mi = lane < NSPLIT * NCONS ? __uint_as_float(ml[0]) : -INFINITY;
    const float li = lane < NSPLIT * NCONS ? __uint_as_float(ml[2]) : 0.f;
    const float M = wave_max(mi);
    const float Lh = wave_sum(li * __builtin_amdgcn_exp2f(mi - M));
    f = __builtin_amdgcn_exp2f(mw * C2 - M) / Lh;
  }
  LAS float* red = (LAS float*)(L + L_RED);
  if (gate_wave) {
    float z = 0.f;
#pragma unroll
    for (int i = 0; i < 5; ++i) {
      z += bf16_rnd(__uint_as_float(gx[i] << 16) * __uint_as_float(gg[i] << 16));
      z += bf16_rnd(__uint_as_float(gx[i] & 0xffff0000u) * __uint_as_float(gg[i] & 0xffff0000u));
    }
    z = bf16_rnd(wave_sum(z));
    const float g = bf16_rnd(1.f / (1.f + __expf(-z)));
    if (lane == 0) red[128] = g;
  }
#pragma unroll 1
  for (; v < NUK; v += NCONS, ++k) vstep(v, k);
  STAMP(5);
  {
#pragma unroll
    for (int e = 0; e < 8; ++e) acc[e] = x32_sum(x16_sum(acc[e])) * f;
    LAS float* part = (LAS float*)(L + L_X);
    if (r == 0) {
      *(LAS v4f*)(part + c * DH + 8 * c16) = v4f{acc[0], acc[1], acc[2], acc[3]};
      *(LAS v4f*)(part + c * DH + 8 * c16 + 4) = v4f{acc[4], acc[5], acc[6], acc[7]};
    }
  }
  STAMP(6);
  hbar();
  if (ct < DH) {
    const LAS float* part = (const LAS float*)(L + L_X);
    float s0 = 0.f;
#pragma unroll
    for (int w = 0; w < NCONS; ++w) s0 += part[w * DH + ct];
    ((LAS unsigned*)(L + L_Q))[(ct & 7) * 16 + (ct >> 3)] = bf16_hi(s0);
  }
  hbar();
  {
    float y[8];
#pragma unroll
    for (int e = 0; e < 8; ++e) y[e] = 0.f;
#pragma unroll
    for (int i4 = 0; i4 < 4; ++i4) {
      const v4u o4 = *(const LAS v4u*)(L + L_Q + wa * 64 + i4 * 16);
#pragma unroll
      for (int ii = 0; ii < 4; ++ii) {
        const unsigned ohi = o4[ii], olo = ohi >> 16;
        const int i = 4 * i4 + ii;
#pragma unroll
        for (int j = 0; j < 4; ++j) { y[2 * j] = dot2(W[i][j], olo, y[2 * j]); y[2 * j + 1] = dot2(W[i][j], ohi, y[2 * j + 1]); }
      }
    }
    LAS float* yr = (LAS float*)(L + L_RING);
    *(LAS v4f*)(yr + wa * DM + 8 * wb) = v4f{y[0], y[1], y[2], y[3]};
    *(LAS v4f*)(yr + wa * DM + 8 * wb + 4) = v4f{y[4], y[5], y[6], y[7]};
  }
  STAMP(7);
  hbar();
  {
    const LAS float* yr = (const LAS float*)(L + L_RING);
    float s0 = 0.f;
#pragma unroll
    for (int a = 0; a < 8; ++a) s0 += yr[a * DM + ct];
    const unsigned long long g = ((unsigned long long)tag << 32) | __float_as_uint(s0);
    __hip_atomic_store((GAS unsigned long long*)(p.ws + WS_Y) + (size_t)(hh * NSPLIT + sp) * DM + ct, g, __ATOMIC_RELAXED, __HIP_MEMORY_SCOPE_AGENT);
  }
  STAMP(8);
  if (b >= NRED) { STAMP_FLUSH(); return; }

  if (c < 8) {
    const int j = lane & 7, gi = lane >> 3;
    const int off0 = WS_Y + ((c * 32 + gi) * DM + b * 16 + 2 * j) * 8;
    v4u g0, g1, g2, g3;
    bool ok = false;
    unsigned spn = 0;
    for (;;) {
      if (!ok) {
        g0 = __builtin_amdgcn_raw_buffer_load_b128(wsr, off0, 0, 16);
        g1 = __builtin_amdgcn_raw_buffer_load_b128(wsr, off0 + 8 * DM * 8, 0, 16);
        g2 = __builtin_amdgcn_raw_buffer_load_b128(wsr, off0 + 16 * DM * 8, 0, 16);
        g3 = __builtin_amdgcn_raw_buffer_load_b128(wsr, off0 + 24 * DM * 8, 0, 16);
        ok = g0[1] == tag && g0[3] == tag && g1[1] == tag && g1[3] == tag && g2[1] == tag && g2[3] == tag && g3[1] == tag && g3[3] == tag;
      }
      if (__all(ok)) break;
      __builtin_amdgcn_s_sleep(4);
      if (++spn > SPIN_GLB) break;
    }
    float s0 = (__uint_as_float(g0[0]) + __uint_as_float(g1[0])) + (__uint_as_float(g2[0]) + __uint_as_float(g3[0]));
    float s1 = (__uint_as_float(g0[2]) + __uint_as_float(g1[2])) + (__uint_as_float(g2[2]) + __uint_as_float(g3[2]));
    s0 += dpp<0x128>(s0); s1 += dpp<0x128>(s1);
    s0 = x32_sum(x16_sum(s0)); s1 = x32_sum(x16_sum(s1));
    if (lane < 8) *(LAS v2f*)(red + c * 16 + 2 * j) = v2f{s0, s1};
  }
  STAMP(9);
  hbar();
  if (ct < 8) {
    const float g = red[128], g1 = bf16_rnd(1.f - g);
    unsigned o2[2];
#pragma unroll
    for (int e = 0; e < 2; ++e) {
      const int col = 2 * ct + e;
      const float a = bf16_rnd((red[col] + red[16 + col]) + (red[32 + col] + red[48 + col]));
      const float bb = bf16_rnd((red[64 + col] + red[80 + col]) + (red[96 + col] + red[112 + col]));
      o2[e] = bf16_hi(bf16_rnd(g * a) + bf16_rnd(g1 * bb));
    }
    ((unsigned*)p.out)[b * 8 + ct] = (o2[0] >> 16) | o2[1];
    if (b == 0 && ct == 0) __hip_atomic_store((GAS unsigned*)(p.ws + WS_EPOCH), tag, __ATOMIC_RELAXED, __HIP_MEMORY_SCOPE_AGENT);
  }
  STAMP(10);
  STAMP_FLUSH();
}
}

extern "C" void kernel_launch(void* const* d_in, const int* in_sizes, int n_in, void* d_out, int out_size, void* d_ws, size_t ws_size,
                              hipStream_t stream) {
  Params p;
  p.x = (const unsigned short*)d_in[0];
  p.k0 = (const unsigned short*)d_in[1]; p.v0 = (const unsigned short*)d_in[2];
  p.wq0 = (const unsigned short*)d_in[3]; p.wo0 = (const unsigned short*)d_in[4];
  p.k1 = (const unsigned short*)d_in[6]; p.v1 = (const unsigned short*)d_in[7];
  p.wq1 = (const unsigned short*)d_in[8]; p.wo1 = (const unsigned short*)d_in[9];
  p.gate = (const unsigned short*)d_in[10];
  p.out = (unsigned short*)d_out;
  p.ws = (unsigned char*)d_ws;
  hipLaunchKernelGGL(fused_attn, dim3(NWG), dim3(NTHR), 0, stream, p);
}
```

```cpp
#include <hip/hip_runtime.h>
#include <hip/hip_bf16.h>
#include <stdint.h>

#define GAS __attribute__((address_space(1)))
#define LAS __attribute__((address_space(3)))
typedef unsigned v4u __attribute__((ext_vector_type(4)));
typedef unsigned v2u __attribute__((ext_vector_type(2)));
typedef float v4f __attribute__((ext_vector_type(4)));
typedef float v2f __attribute__((ext_vector_type(2)));
typedef __bf16 bf16x2 __attribute__((ext_vector_type(2)));

namespace {
constexpr int DM = 640, NH = 32, DH = 128, SK = 16384;
constexpr int NSPLIT = 8, NWG = NH * NSPLIT;
constexpr int NCONS = 10, NTHR = 64 * (NCONS + 1);
constexpr int TOK = SK / NSPLIT;
constexpr int UNIT_B = 4096;
constexpr int NP = TOK / 16, NU = 4 * NP;
#define RING_U 36
#define WIN_A 24
#define WIN_B 24
#define PRE_UNITS 2
#define WO_GAP 2
constexpr int MAXK = (NP + NCONS - 1) / NCONS;
constexpr int NML = NSPLIT * NCONS;
constexpr int NRED = 40;
constexpr float C2 = 0.08838834764831845f * 1.4426950408889634f;
constexpr unsigned SPIN_LDS = 1u << 22, SPIN_GLB = 1u << 18;

constexpr int L_RING = 0;
constexpr int L_SC = RING_U * UNIT_B;
constexpr int L_CTL = L_SC + NCONS * MAXK * 64;
constexpr int CTL_FULL = 0, CTL_BAR = 1, CTL_ABORT = 3, CTL_PROG = 4  , CTL_WORDS = 16;
constexpr int L_X = L_CTL + CTL_WORDS * 4;
constexpr int L_Q = L_X + NCONS * DH * 4;
constexpr int L_RED = L_Q + 512;
constexpr int LDS_BYTES = L_RED + 2 * 8 * 16 * 4 + 16;
static_assert(LDS_BYTES <= 160 * 1024, "LDS");
static_assert(L_SC % 16 == 0 && L_CTL % 16 == 0 && L_X % 16 == 0 && L_Q % 16 == 0 && L_RED % 16 == 0, "align");
static_assert(NCONS * MAXK * 64 >= NCONS * DH * 4, "q partials of branch b alias the score region");
constexpr int WS_EPOCH = 0;
constexpr int WS_ML = 256;
constexpr int WS_Q = WS_ML + 2 * NH * NML * 16;
constexpr int WS_Y = WS_Q + 2 * NH * NSPLIT * DH * 8;
constexpr int WS_END = WS_Y + 2 * NWG * DM * 8;
static_assert(WS_Y % 128 == 0 && WS_Q % 128 == 0, "line aligned");

#define STAMP(i) do {} while (0)
#define STAMPV(i, v) do {} while (0)
#define STAMP_FLUSH() do {} while (0)

typedef const unsigned short* cptr;
struct Params {
  cptr k0, k1, v0, v1, wq0, wq1, wo0, wo1, x, gate;
  unsigned short* out; unsigned char* ws;
};

__device__ __forceinline__ float dot2(unsigned a, unsigned b, float c) {
  return __builtin_amdgcn_fdot2_f32_bf16(__builtin_bit_cast(bf16x2, a), __builtin_bit_cast(bf16x2, b), c, false);
}
template <int CTRL> __device__ __forceinline__ float dpp(float x) {
  return __builtin_bit_cast(float, __builtin_amdgcn_mov_dpp(__builtin_bit_cast(int, x), CTRL, 0xf, 0xf, true));
}
__device__ __forceinline__ float row16_sum(float x) {
  x += dpp<0x128>(x); x += dpp<0x124>(x); x += dpp<0x122>(x); x += dpp<0x121>(x); return x;
}
__device__ __forceinline__ float x16_sum(float x) {
  auto s = __builtin_amdgcn_permlane16_swap(__float_as_uint(x), __float_as_uint(x), false, false);
  return __uint_as_float(s[0]) + __uint_as_float(s[1]);
}
__device__ __forceinline__ float x32_sum(float x) {
  auto s = __builtin_amdgcn_permlane32_swap(__float_as_uint(x), __float_as_uint(x), false, false);
  return __uint_as_float(s[0]) + __uint_as_float(s[1]);
}
__device__ __forceinline__ float x16_max(float x) {
  auto s = __builtin_amdgcn_permlane16_swap(__float_as_uint(x), __float_as_uint(x), false, false);
  return fmaxf(__uint_as_float(s[0]), __uint_as_float(s[1]));
}
__device__ __forceinline__ float x32_max(float x) {
  auto s = __builtin_amdgcn_permlane32_swap(__float_as_uint(x), __float_as_uint(x), false, false);
  return fmaxf(__uint_as_float(s[0]), __uint_as_float(s[1]));
}
__device__ __forceinline__ float wave_sum(float x) { return x32_sum(x16_sum(row16_sum(x))); }
__device__ __forceinline__ float wave_max(float x) {
  x = fmaxf(x, dpp<0x128>(x)); x = fmaxf(x, dpp<0x124>(x)); x = fmaxf(x, dpp<0x122>(x)); x = fmaxf(x, dpp<0x121>(x));
  return x32_max(x16_max(x));
}
__device__ __forceinline__ unsigned bf16_hi(float f) {
  unsigned u = __float_as_uint(f);
  u += 0x7fffu + ((u >> 16) & 1u);
  return u & 0xffff0000u;
}
__device__ __forceinline__ float bf16_rnd(float f) { return __uint_as_float(bf16_hi(f)); }
__device__ __forceinline__ v4u ldnt(const GAS v4u* p) { return __builtin_nontemporal_load(p); }
__device__ __forceinline__ void dma4(unsigned lds_dst, unsigned voff, const void* src) {
  asm volatile(
      "s_mov_b32 m0, %0\n\ts_nop 0\n\t"
      "global_load_lds_dwordx4 %1, %2 nt\n\t"
      "global_load_lds_dwordx4 %1, %2 offset:1024 nt\n\t"
      "global_load_lds_dwordx4 %1, %2 offset:2048 nt\n\t"
      "global_load_lds_dwordx4 %1, %2 offset:3072 nt"
      :: "s"(lds_dst), "v"(voff), "s"(src) : "memory");
}
template <int N> __device__ __forceinline__ void wait_vm() { asm volatile("s_waitcnt vmcnt(%0)" :: "i"(N) : "memory"); }
__device__ __forceinline__ void wait_lgkm() { asm volatile("s_waitcnt lgkmcnt(0)" ::: "memory"); }
__device__ __forceinline__ void hbar() { asm volatile("s_waitcnt lgkmcnt(0)\n\ts_barrier" ::: "memory"); }

typedef volatile LAS unsigned* ctl_t;
__device__ __forceinline__ void wait_full(ctl_t ctl, unsigned u) {
  unsigned sp = 0;
  while (ctl[CTL_FULL] <= u) { __builtin_amdgcn_s_sleep(1); if (++sp > SPIN_LDS) { ctl[CTL_ABORT] = 1u; break; } }
  asm volatile("" ::: "memory");
}
__device__ __forceinline__ unsigned min_prog(ctl_t ctl) {
  const v4u a = *(volatile LAS v4u*)(ctl + CTL_PROG), c = *(volatile LAS v4u*)(ctl + CTL_PROG + 4), d = *(volatile LAS v4u*)(ctl + CTL_PROG + 8);
  const unsigned mn = min(min(min(a.x, a.y), min(a.z, a.w)), min(min(min(c.x, c.y), min(c.z, c.w)), min(min(d.x, d.y), min(d.z, d.w))));
  return __builtin_amdgcn_readfirstlane(mn);
}
__device__ __forceinline__ void cbar(ctl_t ctl, unsigned& gen, int lane) {
  wait_lgkm();
  gen += NCONS;
  if (lane == 0) __hip_atomic_fetch_add((LAS unsigned*)(ctl + CTL_BAR), 1u, __ATOMIC_RELAXED, __HIP_MEMORY_SCOPE_WORKGROUP);
  unsigned sp = 0;
  while (ctl[CTL_BAR] < gen) { if (++sp > SPIN_LDS) { ctl[CTL_ABORT] = 1u; break; } }
  asm volatile("" ::: "memory");
}
#define TAGS_OK(g) ((g)[1] == tag && (g)[3] == tag)

struct Loader {
  ctl_t ctl; unsigned ring, voff, free_upto, slot; int landed;
  template <int WIN> __device__ __forceinline__ void pass(const unsigned char* src, unsigned u0, unsigned u1) {
    constexpr int LAG = (WIN - 4) / 4;
    for (unsigned u = u0; u < u1; ++u) {
      if (u + 4 >= free_upto) {
        free_upto = min_prog(ctl) + RING_U;
        if (u >= free_upto) {
          wait_vm<0>();
          landed = (int)u;
          ctl[CTL_FULL] = u;
          unsigned spn = 0;
          while (u >= free_upto) {
            __builtin_amdgcn_s_sleep(1);
            free_upto = min_prog(ctl) + RING_U;
            if (++spn > SPIN_LDS) { ctl[CTL_ABORT] = 1u; free_upto = 0xffffffu; }
          }
        }
      }
      dma4(ring + slot * UNIT_B, voff, src);
      src += UNIT_B;
      slot = (slot + 1 == RING_U) ? 0u : slot + 1;
      wait_vm<WIN - 4>();
      landed = max(landed, (int)u + 1 - LAG);
      ctl[CTL_FULL] = (unsigned)landed;
    }
  }
};

__global__ void __launch_bounds__(NTHR) fused_attn(Params p) {
  __shared__ __attribute__((aligned(16))) unsigned char smem[LDS_BYTES];
  LAS unsigned char* L = (LAS unsigned char*)smem;
  ctl_t ctl = (ctl_t)(L + L_CTL);
  const int tid = threadIdx.x, lane = tid & 63, wave = __builtin_amdgcn_readfirstlane(tid >> 6);
  const int b = blockIdx.x, h = b & 31, sp = b >> 5;
  const size_t kv_off = ((size_t)h * SK + (size_t)sp * TOK) * DH;
  {
    cptr k0 = p.k0, k1 = p.k1, v0 = p.v0, v1 = p.v1, q0 = p.wq0, q1 = p.wq1, o0 = p.wo0, o1 = p.wo1, x_ = p.x, g_ = p.gate;
    unsigned short* out_ = p.out; unsigned char* ws_ = p.ws;
    asm volatile("" : "+s"(k0), "+s"(k1), "+s"(v0), "+s"(v1), "+s"(q0), "+s"(q1), "+s"(o0), "+s"(o1), "+s"(x_), "+s"(g_), "+s"(out_), "+s"(ws_));
    p.k0 = k0; p.k1 = k1; p.v0 = v0; p.v1 = v1; p.wq0 = q0; p.wq1 = q1; p.wo0 = o0; p.wo1 = o1; p.x = x_; p.gate = g_; p.out = out_; p.ws = ws_;
  }
  if (wave == 0) {
    __builtin_amdgcn_s_setprio(3);
    STAMP(0);
    Loader ld;
    ld.ctl = ctl; ld.ring = (unsigned)(uintptr_t)(L + L_RING); ld.voff = lane * 16; ld.free_upto = RING_U; ld.slot = PRE_UNITS; ld.landed = 0;
    const unsigned char* ka = (const unsigned char*)(p.k0 + kv_off);
    if (lane < CTL_WORDS) ctl[lane] = (lane >= CTL_PROG && lane < CTL_PROG + NCONS) ? (unsigned)(lane - CTL_PROG) : (lane >= CTL_PROG + NCONS ? 0xffffffffu : 0u);
#pragma unroll
    for (int u = 0; u < PRE_UNITS; ++u) dma4(ld.ring + u * UNIT_B, ld.voff, ka + u * UNIT_B);
    wait_lgkm();
    __builtin_amdgcn_s_barrier();
    STAMP(1);
    ld.pass<WIN_A>(ka + PRE_UNITS * UNIT_B, PRE_UNITS, NP);
    STAMP(2);
    ld.pass<WIN_A>((const unsigned char*)(p.v0 + kv_off), NP, 2 * NP);
    STAMP(7);
    ld.pass<WIN_B>((const unsigned char*)(p.k1 + kv_off), 2 * NP, 3 * NP);
    STAMP(8);
    ld.pass<WIN_B>((const unsigned char*)(p.v1 + kv_off), 3 * NP, 4 * NP);
    constexpr int LAGB = (WIN_B - 4) / 4;
#define DRAIN_STEP(K) if (LAGB > (K)) { wait_vm<4 * (K)>(); ctl[CTL_FULL] = NU - (K); }
    DRAIN_STEP(14) DRAIN_STEP(13) DRAIN_STEP(12) DRAIN_STEP(11) DRAIN_STEP(10) DRAIN_STEP(9) DRAIN_STEP(8) DRAIN_STEP(7)
    DRAIN_STEP(6) DRAIN_STEP(5) DRAIN_STEP(4) DRAIN_STEP(3) DRAIN_STEP(2) DRAIN_STEP(1) DRAIN_STEP(0)
#undef DRAIN_STEP
    return;
  }

  const int c = wave - 1, ct = tid - 64;
  const int r = lane >> 4, c16 = lane & 15;
  STAMP(0);
  const __amdgpu_buffer_rsrc_t wsr = __builtin_amdgcn_make_buffer_rsrc(p.ws, 0, WS_END, 0x00020000);
  unsigned tag, gen = 0;
  {
    const int row0 = sp * (DM / NSPLIT) + 2 * (ct >> 4);
    const size_t wqo = (size_t)row0 * (NH * DH) + h * DH + 8 * c16;
    v4u w4[4];
    w4[0] = ldnt((const GAS v4u*)(p.wq0 + wqo)); w4[1] = ldnt((const GAS v4u*)(p.wq0 + wqo + NH * DH));
    w4[2] = ldnt((const GAS v4u*)(p.wq1 + wqo)); w4[3] = ldnt((const GAS v4u*)(p.wq1 + wqo + NH * DH));
    const unsigned xd = *(const GAS unsigned*)(p.x + row0);
    const unsigned ep = __hip_atomic_load((GAS unsigned*)(p.ws + WS_EPOCH), __ATOMIC_RELAXED, __HIP_MEMORY_SCOPE_AGENT);
    __builtin_amdgcn_sched_barrier(0);
    __builtin_amdgcn_s_barrier();
    __builtin_amdgcn_sched_barrier(0);
    const unsigned x0l = xd & 0xffffu, x0h = xd << 16, x1l = xd >> 16, x1h = xd & 0xffff0000u;
#pragma unroll
    for (int bb = 0; bb < 2; ++bb) {
      float qa[8];
#pragma unroll
      for (int j = 0; j < 4; ++j) {
        qa[2 * j] = dot2(w4[2 * bb + 1][j], x1l, dot2(w4[2 * bb][j], x0l, 0.f));
        qa[2 * j + 1] = dot2(w4[2 * bb + 1][j], x1h, dot2(w4[2 * bb][j], x0h, 0.f));
      }
#pragma unroll
      for (int e = 0; e < 8; ++e) qa[e] = x32_sum(x16_sum(qa[e]));
      LAS float* part = (LAS float*)(L + (bb ? L_SC : L_X));
      if (r == 0) {
        *(LAS v4f*)(part + c * DH + 8 * c16) = v4f{qa[0], qa[1], qa[2], qa[3]};
        *(LAS v4f*)(part + c * DH + 8 * c16 + 4) = v4f{qa[4], qa[5], qa[6], qa[7]};
      }
    }
    tag = __builtin_amdgcn_readfirstlane(ep) * 1664525u + 1013904223u;
  }
  STAMP(1);
  cbar(ctl, gen, lane);
  if (c < 2) {
    const LAS float* part = (const LAS float*)(L + (c ? L_SC : L_X));
    float s0 = 0.f, s1 = 0.f;
#pragma unroll
    for (int w = 0; w < NCONS; ++w) { const v2f t = *(const LAS v2f*)(part + w * DH + 2 * lane); s0 += t.x; s1 += t.y; }
    const int qoff = WS_Q + ((c * NH + h) * NSPLIT * DH + 2 * lane) * 8;
    __builtin_amdgcn_raw_buffer_store_b128(v4u{__float_as_uint(s0), tag, __float_as_uint(s1), tag}, wsr, qoff + sp * DH * 8, 0, 16);
    v4u g[NSPLIT];
    unsigned spn = 0;
    for (;;) {
      bool ok = true;
#pragma unroll
      for (int i = 0; i < NSPLIT; ++i) g[i] = __builtin_amdgcn_raw_buffer_load_b128(wsr, qoff + i * DH * 8, 0, 16);
#pragma unroll
      for (int i = 0; i < NSPLIT; ++i) ok = ok && TAGS_OK(g[i]);
      if (__all(ok) || ++spn > SPIN_GLB) break;
      __builtin_amdgcn_s_sleep(1);
    }
    s0 = 0.f; s1 = 0.f;
#pragma unroll
    for (int i = 0; i < NSPLIT; ++i) { s0 += __uint_as_float(g[i][0]); s1 += __uint_as_float(g[i][2]); }
    ((LAS unsigned*)(L + L_Q))[c * 64 + lane] = (bf16_hi(s0) >> 16) | bf16_hi(s1);
  }
  cbar(ctl, gen, lane);
  STAMP(2);

  LAS unsigned char* sc = L + L_SC + c * (MAXK * 64);
  LAS float* red = (LAS float*)(L + L_RED);
  const bool reducer = b < NRED;
  v4u W[16];
#pragma unroll 1
  for (int br = 0; br < 2; ++br) {
    int lane = tid & 63;
    asm volatile("" : "+v"(lane));
    const int r = lane >> 4, c16 = lane & 15;
    const int wa = lane >> 3, wb = 8 * c + (lane & 7);
    const int ct = 64 * c + lane;
    const v4u q = *(const LAS v4u*)(L + L_Q + br * 256 + 16 * c16);
    const GAS v4u* wo = (const GAS v4u*)((br ? p.wo1 : p.wo0) + (size_t)(h * DH + wa) * DM + 8 * wb);
    const int ub = br * 2 * NP;
    float mrun = -INFINITY;
    int nk = 0, j = c;
    auto kstep = [&]() {
      const int u = ub + j;
      wait_full(ctl, u);
      const LAS unsigned char* base = L + L_RING + (u % RING_U) * UNIT_B + lane * 16;
      v4u kk[4];
#pragma unroll
      for (int i = 0; i < 4; ++i) kk[i] = *(const LAS v4u*)(base + i * 1024);
      wait_lgkm();
      asm volatile("" : "+v"(kk[0]), "+v"(kk[1]), "+v"(kk[2]), "+v"(kk[3]));
      ctl[CTL_PROG + c] = (j + NCONS < NP) ? (unsigned)(u + NCONS) : (unsigned)(ub + NP + c);
      float s[4];
#pragma unroll
      for (int i = 0; i < 4; ++i) {
        float d = dot2(kk[i][0], q[0], 0.f); d = dot2(kk[i][1], q[1], d); d = dot2(kk[i][2], q[2], d); d = dot2(kk[i][3], q[3], d);
        s[i] = row16_sum(d);
      }
      mrun = fmaxf(fmaxf(mrun, fmaxf(s[0], s[1])), fmaxf(s[2], s[3]));
      if (c16 == 0) *(LAS v4f*)(sc + nk * 64 + r * 16) = v4f{s[0], s[1], s[2], s[3]};
      j += NCONS; ++nk;
    };
#pragma unroll
    for (int seg = 0; seg < 4; ++seg) {
#pragma unroll 1
      for (int i = 0; i < WO_GAP; ++i) kstep();
#pragma unroll
      for (int i = 4 * seg; i < 4 * seg + 4; ++i) W[i] = ldnt(wo + (size_t)i * (8 * DM / 8));
      __builtin_amdgcn_sched_barrier(0);
    }
#pragma unroll 1
    while (j < NP) kstep();
    if (br == 0) STAMP(3); else STAMP(11);
    const float mw = x32_max(x16_max(mrun));
    {
      float l = 0.f;
      wait_lgkm();
      for (int idx = lane; idx < nk * 16; idx += 64) {
        const float s = ((const LAS float*)sc)[idx];
        const float e = __builtin_amdgcn_exp2f((s - mw) * C2);
        l += e;
        ((LAS unsigned*)sc)[idx] = bf16_hi(e);
      }
      l = wave_sum(l);
      if (lane == 0) {
        const v4u g = {__float_as_uint(mw * C2), tag, __float_as_uint(l), tag};
        __builtin_amdgcn_raw_buffer_store_b128(g, wsr, WS_ML + ((br * NH + h) * NML + sp * NCONS + c) * 16, 0, 16);
      }
      wait_lgkm();
    }
    auto gather_y = [&](int bsel) {
      const int yj = lane & 7;
      const int off = WS_Y + ((bsel * NWG + c * 32 + (lane >> 3)) * DM + b * 16 + 2 * yj) * 8;
      v4u g0, g1, g2, g3;
      bool ok = false;
      unsigned spn = 0;
      for (;;) {
        if (!ok) {
          g0 = __builtin_amdgcn_raw_buffer_load_b128(wsr, off, 0, 16);
          g1 = __builtin_amdgcn_raw_buffer_load_b128(wsr, off + 8 * DM * 8, 0, 16);
          g2 = __builtin_amdgcn_raw_buffer_load_b128(wsr, off + 16 * DM * 8, 0, 16);
          g3 = __builtin_amdgcn_raw_buffer_load_b128(wsr, off + 24 * DM * 8, 0, 16);
          ok = TAGS_OK(g0) && TAGS_OK(g1) && TAGS_OK(g2) && TAGS_OK(g3);
        }
        if (__all(ok) || ++spn > SPIN_GLB) break;
        __builtin_amdgcn_s_sleep(4);
      }
      float s0 = (__uint_as_float(g0[0]) + __uint_as_float(g1[0])) + (__uint_as_float(g2[0]) + __uint_as_float(g3[0]));
      float s1 = (__uint_as_float(g0[2]) + __uint_as_float(g1[2])) + (__uint_as_float(g2[2]) + __uint_as_float(g3[2]));
      s0 += dpp<0x128>(s0); s1 += dpp<0x128>(s1);
      s0 = x32_sum(x16_sum(s0)); s1 = x32_sum(x16_sum(s1));
      if (lane < 8) *(LAS v2f*)(red + bsel * 128 + c * 16 + 2 * yj) = v2f{s0, s1};
    };
    if (br == 1 && reducer) {
      if (c < 8) gather_y(0);
      else if (c == 8) {
        float z = 0.f;
#pragma unroll
        for (int i = 0; i < 5; ++i) {
          const unsigned gx = ((const GAS unsigned*)p.x)[lane + 64 * i], gg = ((const GAS unsigned*)p.gate)[lane + 64 * i];
          z += bf16_rnd(__uint_as_float(gx << 16) * __uint_as_float(gg << 16));
          z += bf16_rnd(__uint_as_float(gx & 0xffff0000u) * __uint_as_float(gg & 0xffff0000u));
        }
        z = bf16_rnd(wave_sum(z));
        const float g = bf16_rnd(1.f / (1.f + __expf(-z)));
        if (lane == 0) red[256] = g;
      }
    }
    float acc[8];
#pragma unroll
    for (int e = 0; e < 8; ++e) acc[e] = 0.f;
    int k = 0;
    j = c;
    auto vstep = [&]() {
      const int u = ub + NP + j;
      wait_full(ctl, u);
      const LAS unsigned char* base = L + L_RING + (u % RING_U) * UNIT_B + lane * 16;
      v4u vv[4];
#pragma unroll
      for (int i = 0; i < 4; ++i) vv[i] = *(const LAS v4u*)(base + i * 1024);
      v4u pp = *(const LAS v4u*)(sc + k * 64 + r * 16);
      wait_lgkm();
      asm volatile("" : "+v"(vv[0]), "+v"(vv[1]), "+v"(vv[2]), "+v"(vv[3]), "+v"(pp));
      ctl[CTL_PROG + c] = (j + NCONS < NP) ? (unsigned)(u + NCONS) : (br == 0 ? (unsigned)(2 * NP + c) : 0xffffffffu);
#pragma unroll
      for (int i = 0; i < 4; ++i) {
        const unsigned phi = pp[i], plo = phi >> 16;
#pragma unroll
        for (int jj = 0; jj < 4; ++jj) { acc[2 * jj] = dot2(vv[i][jj], plo, acc[2 * jj]); acc[2 * jj + 1] = dot2(vv[i][jj], phi, acc[2 * jj + 1]); }
      }
      j += NCONS; ++k;
    };
#pragma unroll 1
    while (k < 6) vstep();
    const int ml_off = WS_ML + ((br * NH + h) * NML + lane) * 16, ml_off2 = WS_ML + ((br * NH + h) * NML + 64 + (lane & 15)) * 16;
    v4u ml = __builtin_amdgcn_raw_buffer_load_b128(wsr, ml_off, 0, 16);
    v4u ml2 = __builtin_amdgcn_raw_buffer_load_b128(wsr, ml_off2, 0, 16);
#pragma unroll 1
    while (k < 9) vstep();
    float f;
    {
      unsigned spn = 0;
      while (!__all(TAGS_OK(ml) && TAGS_OK(ml2))) {
        __builtin_amdgcn_s_sleep(2);
        ml = __builtin_amdgcn_raw_buffer_load_b128(wsr, ml_off, 0, 16);
        ml2 = __builtin_amdgcn_raw_buffer_load_b128(wsr, ml_off2, 0, 16);
        if (++spn > SPIN_GLB) break;
      }
      const float m1 = __uint_as_float(ml[0]), m2 = lane < 16 ? __uint_as_float(ml2[0]) : -INFINITY;
      const float M = wave_max(fmaxf(m1, m2));
      const float l1 = __uint_as_float(ml[2]) * __builtin_amdgcn_exp2f(m1 - M);
      const float l2 = lane < 16 ? __uint_as_float(ml2[2]) * __builtin_amdgcn_exp2f(m2 - M) : 0.f;
      const float Lh = wave_sum(l1 + l2);
      f = __builtin_amdgcn_exp2f(mw * C2 - M) / Lh;
    }
#pragma unroll 1
    while (j < NP) vstep();
    if (br == 0) STAMP(4); else STAMP(5);
    {
#pragma unroll
      for (int e = 0; e < 8; ++e) acc[e] = x32_sum(x16_sum(acc[e])) * f;
      LAS float* part = (LAS float*)(L + L_X);
      if (r == 0) {
        *(LAS v4f*)(part + c * DH + 8 * c16) = v4f{acc[0], acc[1], acc[2], acc[3]};
        *(LAS v4f*)(part + c * DH + 8 * c16 + 4) = v4f{acc[4], acc[5], acc[6], acc[7]};
      }
    }
    if (br == 0) cbar(ctl, gen, lane); else hbar();
    {
      const LAS float* part = (const LAS float*)(L + L_X);
      float s0 = 0.f, s1 = 0.f;
#pragma unroll
      for (int w = 0; w < NCONS; ++w) { const v2f t = *(const LAS v2f*)(part + w * DH + 2 * lane); s0 += t.x; s1 += t.y; }
      LAS unsigned* ot = (LAS unsigned*)sc;
      ot[((2 * lane) & 7) * 16 + (lane >> 2)] = bf16_hi(s0);
      ot[((2 * lane + 1) & 7) * 16 + (lane >> 2)] = bf16_hi(s1);
    }
    float y[8];
#pragma unroll
    for (int e = 0; e < 8; ++e) y[e] = 0.f;
#pragma unroll
    for (int i4 = 0; i4 < 4; ++i4) {
      const v4u o4 = *(const LAS v4u*)(sc + wa * 64 + i4 * 16);
#pragma unroll
      for (int ii = 0; ii < 4; ++ii) {
        const unsigned ohi = o4[ii], olo = ohi >> 16;
        const int i = 4 * i4 + ii;
#pragma unroll
        for (int jj = 0; jj < 4; ++jj) { y[2 * jj] = dot2(W[i][jj], olo, y[2 * jj]); y[2 * jj + 1] = dot2(W[i][jj], ohi, y[2 * jj + 1]); }
      }
    }
    {
      float z[4];
#pragma unroll
      for (int i = 0; i < 4; ++i) {
        auto t = __builtin_amdgcn_permlane32_swap(__float_as_uint(y[i]), __float_as_uint(y[i + 4]), false, false);
        z[i] = __uint_as_float(t[0]) + __uint_as_float(t[1]);
      }
      float u2[2];
#pragma unroll
      for (int i = 0; i < 2; ++i) {
        auto t = __builtin_amdgcn_permlane16_swap(__float_as_uint(z[i]), __float_as_uint(z[i + 2]), false, false);
        u2[i] = __uint_as_float(t[0]) + __uint_as_float(t[1]);
      }
      const bool odd = (lane & 8) != 0;
      const float keep = odd ? u2[1] : u2[0], send = odd ? u2[0] : u2[1];
      const float tot = keep + dpp<0x128>(send);
      const unsigned long long g = ((unsigned long long)tag << 32) | __float_as_uint(tot);
      __hip_atomic_store((GAS unsigned long long*)(p.ws + WS_Y) + (size_t)(br * NWG + h * NSPLIT + sp) * DM + 8 * wb + wa, g, __ATOMIC_RELAXED, __HIP_MEMORY_SCOPE_AGENT);
    }
    if (br == 0) STAMP(6); else STAMP(8);
    if (br == 1 && reducer) {
      if (c < 8) gather_y(1);
      STAMP(9);
      hbar();
      if (ct < 8) {
        const float g = red[256], g1 = bf16_rnd(1.f - g);
        unsigned o2[2];
#pragma unroll
        for (int e = 0; e < 2; ++e) {
          const int col = 2 * ct + e;
          float a = 0.f, bb = 0.f;
#pragma unroll
          for (int w = 0; w < 8; ++w) { a += red[w * 16 + col]; bb += red[128 + w * 16 + col]; }
          o2[e] = bf16_hi(bf16_rnd(g * bf16_rnd(a)) + bf16_rnd(g1 * bf16_rnd(bb)));
        }
        ((unsigned*)p.out)[b * 8 + ct] = (o2[0] >> 16) | o2[1];
        if (b == 0 && ct == 0) __hip_atomic_store((GAS unsigned*)(p.ws + WS_EPOCH), tag, __ATOMIC_RELAXED, __HIP_MEMORY_SCOPE_AGENT);
      }
      STAMP(10);
    }
  }
  STAMP_FLUSH();
}
}

extern "C" void kernel_launch(void* const* d_in, const int* in_sizes, int n_in, void* d_out, int out_size, void* d_ws, size_t ws_size,
                              hipStream_t stream) {
  Params p;
  p.x = (const unsigned short*)d_in[0];
  p.k0 = (const unsigned short*)d_in[1]; p.v0 = (const unsigned short*)d_in[2];
  p.wq0 = (const unsigned short*)d_in[3]; p.wo0 = (const unsigned short*)d_in[4];
  p.k1 = (const unsigned short*)d_in[6]; p.v1 = (const unsigned short*)d_in[7];
  p.wq1 = (const unsigned short*)d_in[8]; p.wo1 = (const unsigned short*)d_in[9];
  p.gate = (const unsigned short*)d_in[10];
  p.out = (unsigned short*)d_out;
  p.ws = (unsigned char*)d_ws;
  hipLaunchKernelGGL(fused_attn, dim3(NWG), dim3(NTHR), 0, stream, p);
}
```

```cpp
#include <hip/hip_runtime.h>
#include <hip/hip_bf16.h>
#include <stdint.h>

#define GAS __attribute__((address_space(1)))
#define LAS __attribute__((address_space(3)))
typedef unsigned v4u __attribute__((ext_vector_type(4)));
typedef unsigned v2u __attribute__((ext_vector_type(2)));
typedef float v4f __attribute__((ext_vector_type(4)));
typedef float v2f __attribute__((ext_vector_type(2)));
typedef __bf16 bf16x2 __attribute__((ext_vector_type(2)));

namespace {
constexpr int DM = 640, NH = 32, DH = 128, SK = 16384;
constexpr int NSPLIT = 4, NWG = 2 * NH * NSPLIT;
constexpr int NCONS = 10, NTHR = 64 * (NCONS + 1);
constexpr int TOK = SK / NSPLIT;
constexpr int UNIT_B = 4096;
constexpr int NP = TOK / 16, NU = 2 * NP;
constexpr int RING_U = 34;
constexpr int INFL_MIN = 3, INFL_MID = 5, INFL_MAX = 13;
constexpr int LAG_LO = -100, LAG_HI = 300, LAG_C0 = 100;
constexpr unsigned TAU16 = 221;
constexpr int MAXK = (NP + NCONS - 1) / NCONS;
constexpr int NRED = 40;
constexpr float C2 = 0.08838834764831845f * 1.4426950408889634f;
constexpr unsigned SPIN_LDS = 1u << 22, SPIN_GLB = 1u << 18;

constexpr int L_RING = 0;
constexpr int L_SC = RING_U * UNIT_B;
constexpr int L_CTL = L_SC + NCONS * MAXK * 64;
constexpr int CTL_FULL = 0, CTL_BAR = 1, CTL_WQ = 2  , CTL_ABORT = 3, CTL_PROG = 4  , CTL_WORDS = 16;
constexpr int WQ_UNITS = 10, WQ_SLOT = RING_U - WQ_UNITS;
constexpr int PRE_UNITS = 5;
constexpr int L_X = L_CTL + CTL_WORDS * 4;
constexpr int L_ML = L_X + NCONS * DH * 4;
constexpr int L_F = L_ML + 96;
constexpr int L_Q = L_F + 16;
constexpr int L_RED = L_Q + 256;
constexpr int LDS_BYTES = L_RED + 8 * 16 * 4 + 16;
static_assert(LDS_BYTES <= 160 * 1024, "LDS");
static_assert(50 - RING_U >= NCONS && 200 - RING_U >= 13 * NCONS + NCONS, "progress arguments in the V pass");
static_assert(L_SC % 16 == 0 && L_CTL % 16 == 0 && L_X % 16 == 0 && L_Q % 16 == 0 && L_RED % 16 == 0, "align");
constexpr int WS_EPOCH = 0;
constexpr int WS_ML = 256;
constexpr int WS_Q = WS_ML + 2 * NH * NSPLIT * 16;
constexpr int WS_O = WS_Q;
constexpr int WS_Y = WS_Q + 2 * NH * NSPLIT * DH * 8;
constexpr int WS_END = WS_Y + NWG * DM * 8;
static_assert(WS_Y % 128 == 0 && WS_Q % 128 == 0, "line aligned");

typedef const unsigned short* cptr;
struct Params {
  cptr k0, k1, v0, v1, wq0, wq1, wo0, wo1, x, gate;
  unsigned short* out; unsigned char* ws;
};

__device__ __forceinline__ float dot2(unsigned a, unsigned b, float c) {
  return __builtin_amdgcn_fdot2_f32_bf16(__builtin_bit_cast(bf16x2, a), __builtin_bit_cast(bf16x2, b), c, false);
}
template <int CTRL> __device__ __forceinline__ float dpp(float x) {
  return __builtin_bit_cast(float, __builtin_amdgcn_mov_dpp(__builtin_bit_cast(int, x), CTRL, 0xf, 0xf, true));
}
__device__ __forceinline__ float row16_sum(float x) {
  x += dpp<0x128>(x); x += dpp<0x124>(x); x += dpp<0x122>(x); x += dpp<0x121>(x); return x;
}
__device__ __forceinline__ float x16_sum(float x) {
  auto s = __builtin_amdgcn_permlane16_swap(__float_as_uint(x), __float_as_uint(x), false, false);
  return __uint_as_float(s[0]) + __uint_as_float(s[1]);
}
__device__ __forceinline__ float x32_sum(float x) {
  auto s = __builtin_amdgcn_permlane32_swap(__float_as_uint(x), __float_as_uint(x), false, false);
  return __uint_as_float(s[0]) + __uint_as_float(s[1]);
}
__device__ __forceinline__ float x16_max(float x) {
  auto s = __builtin_amdgcn_permlane16_swap(__float_as_uint(x), __float_as_uint(x), false, false);
  return fmaxf(__uint_as_float(s[0]), __uint_as_float(s[1]));
}
__device__ __forceinline__ float x32_max(float x) {
  auto s = __builtin_amdgcn_permlane32_swap(__float_as_uint(x), __float_as_uint(x), false, false);
  return fmaxf(__uint_as_float(s[0]), __uint_as_float(s[1]));
}
__device__ __forceinline__ float wave_sum(float x) { return x32_sum(x16_sum(row16_sum(x))); }
__device__ __forceinline__ unsigned bf16_hi(float f) {
  unsigned u = __float_as_uint(f);
  u += 0x7fffu + ((u >> 16) & 1u);
  return u & 0xffff0000u;
}
__device__ __forceinline__ float bf16_rnd(float f) { return __uint_as_float(bf16_hi(f)); }
__device__ __forceinline__ v4u ldnt(const GAS v4u* p) { return __builtin_nontemporal_load(p); }
__device__ __forceinline__ void dma4(unsigned lds_dst, unsigned voff, const void* src) {
  asm volatile(
      "s_mov_b32 m0, %0\n\ts_nop 0\n\t"
      "global_load_lds_dwordx4 %1, %2 nt\n\t"
      "global_load_lds_dwordx4 %1, %2 offset:1024 nt\n\t"
      "global_load_lds_dwordx4 %1, %2 offset:2048 nt\n\t"
      "global_load_lds_dwordx4 %1, %2 offset:3072 nt"
      :: "s"(lds_dst), "v"(voff), "s"(src) : "memory");
}
__device__ __forceinline__ void dma1(unsigned lds_dst, unsigned voff, const void* src) {
  asm volatile("s_mov_b32 m0, %0\n\ts_nop 0\n\tglobal_load_lds_dwordx4 %1, %2 nt" :: "s"(lds_dst), "v"(voff), "s"(src) : "memory");
}
template <int N> __device__ __forceinline__ void wait_vm() { asm volatile("s_waitcnt vmcnt(%0)" :: "i"(N) : "memory"); }
__device__ __forceinline__ void wait_lgkm() { asm volatile("s_waitcnt lgkmcnt(0)" ::: "memory"); }
__device__ __forceinline__ void hbar() { asm volatile("s_waitcnt lgkmcnt(0)\n\ts_barrier" ::: "memory"); }

typedef volatile LAS unsigned* ctl_t;
__device__ __forceinline__ bool give_up(ctl_t ctl, unsigned& sp) {
  if ((++sp & 1023u) != 0u) return false;
  if (sp > SPIN_LDS) ctl[CTL_ABORT] = 1u;
  return ctl[CTL_ABORT] != 0u;
}
__device__ __forceinline__ void wait_full(ctl_t ctl, unsigned u) {
  unsigned sp = 0;
  while (ctl[CTL_FULL] <= u) { __builtin_amdgcn_s_sleep(1); if (give_up(ctl, sp)) break; }
  asm volatile("" ::: "memory");
}
__device__ __forceinline__ unsigned min_prog(ctl_t ctl) {
  const v4u a = *(volatile LAS v4u*)(ctl + CTL_PROG), c = *(volatile LAS v4u*)(ctl + CTL_PROG + 4), d = *(volatile LAS v4u*)(ctl + CTL_PROG + 8);
  const unsigned mn = min(min(min(a.x, a.y), min(a.z, a.w)), min(min(min(c.x, c.y), min(c.z, c.w)), min(min(d.x, d.y), min(d.z, d.w))));
  return __builtin_amdgcn_readfirstlane(mn);
}
__device__ __forceinline__ unsigned ring_limit(ctl_t ctl) {
  const unsigned wq = __builtin_amdgcn_readfirstlane(ctl[CTL_WQ]);
  return min_prog(ctl) + (wq >= 2u ? RING_U : WQ_SLOT);
}
__device__ __forceinline__ void cbar(ctl_t ctl, unsigned& gen, int lane) {
  wait_lgkm();
  gen += NCONS;
  if (lane == 0) __hip_atomic_fetch_add((LAS unsigned*)(ctl + CTL_BAR), 1u, __ATOMIC_RELAXED, __HIP_MEMORY_SCOPE_WORKGROUP);
  unsigned sp = 0;
  while (ctl[CTL_BAR] < gen) { if (give_up(ctl, sp)) break; }
  asm volatile("" ::: "memory");
}
#define TAGS_OK(g) ((g)[1] == tag && (g)[3] == tag)

__device__ __forceinline__ void wait_vm_units(int n) {
  asm volatile(
      "s_getpc_b64 vcc\n\t"
      "s_add_u32 vcc_lo, vcc_lo, %0\n\t"
      "s_addc_u32 vcc_hi, vcc_hi, 0\n\t"
      "s_setpc_b64 vcc\n\t"
      "s_waitcnt vmcnt(0)\n\ts_branch 1f\n\t"  "s_waitcnt vmcnt(4)\n\ts_branch 1f\n\t"  "s_waitcnt vmcnt(8)\n\ts_branch 1f\n\t"  "s_waitcnt vmcnt(12)\n\ts_branch 1f\n\t"
      "s_waitcnt vmcnt(16)\n\ts_branch 1f\n\t" "s_waitcnt vmcnt(20)\n\ts_branch 1f\n\t" "s_waitcnt vmcnt(24)\n\ts_branch 1f\n\t" "s_waitcnt vmcnt(28)\n\ts_branch 1f\n\t"
      "s_waitcnt vmcnt(32)\n\ts_branch 1f\n\t" "s_waitcnt vmcnt(36)\n\ts_branch 1f\n\t" "s_waitcnt vmcnt(40)\n\ts_branch 1f\n\t" "s_waitcnt vmcnt(44)\n\ts_branch 1f\n\t"
      "s_waitcnt vmcnt(48)\n\ts_branch 1f\n\t" "s_waitcnt vmcnt(52)\n\ts_branch 1f\n\t" "s_waitcnt vmcnt(56)\n\ts_branch 1f\n\t" "s_waitcnt vmcnt(60)\n"
      "1:"
      :: "s"(n * 8 + 12) : "vcc", "scc", "memory");
}
struct Loader {
  ctl_t ctl; unsigned ring, voff, free_upto, slot, t0; int landed; unsigned long long tclk;
  __device__ __forceinline__ void pass(const unsigned char* src, unsigned u0, unsigned u1) {
    for (unsigned u = u0; u < u1; ++u) {
      if (u + 4 >= free_upto) {
        free_upto = ring_limit(ctl);
        if (u >= free_upto) {
          wait_vm<0>();
          landed = (int)u;
          ctl[CTL_FULL] = u;
          unsigned spn = 0;
          while (u >= free_upto) {
            __builtin_amdgcn_s_sleep(1);
            free_upto = ring_limit(ctl);
            if (give_up(ctl, spn)) free_upto = 0xffffffu;
          }
        }
      }
      dma4(ring + slot * UNIT_B, voff, src);
      asm volatile("s_waitcnt lgkmcnt(0)" : "+s"(tclk) :: "memory");
      const int lag = (int)((unsigned)tclk - t0) - (int)((u * TAU16) >> 4) - LAG_C0;
      asm volatile("s_memrealtime %0" : "=s"(tclk) :: "memory");
      src += UNIT_B;
      slot = (slot + 1 == RING_U) ? 0u : slot + 1;
      const int infl = lag > LAG_HI ? INFL_MAX : (lag > LAG_LO ? INFL_MID : INFL_MIN);
      wait_vm_units(infl);
      landed = max(landed, (int)u + 1 - infl);
      ctl[CTL_FULL] = (unsigned)landed;
    }
    asm volatile("s_waitcnt lgkmcnt(0)" : "+s"(tclk) :: "memory");
  }
};

__global__ void __launch_bounds__(NTHR) fused_attn(Params p) {
  __shared__ __attribute__((aligned(16))) unsigned char smem[LDS_BYTES];
  LAS unsigned char* L = (LAS unsigned char*)smem;
  ctl_t ctl = (ctl_t)(L + L_CTL);
  const int tid = threadIdx.x, lane = tid & 63, wave = __builtin_amdgcn_readfirstlane(tid >> 6);
  const int b = blockIdx.x, hh = b & 63, sp = b >> 6, br = hh >> 5, h = hh & 31;
  const size_t kv_off = ((size_t)h * SK + (size_t)sp * TOK) * DH;
  cptr pk, pv, pwq, pwo;
  {
    cptr k0 = p.k0, k1 = p.k1, v0 = p.v0, v1 = p.v1, q0 = p.wq0, q1 = p.wq1, o0 = p.wo0, o1 = p.wo1, x_ = p.x, g_ = p.gate;
    unsigned short* out_ = p.out; unsigned char* ws_ = p.ws;
    asm volatile("" : "+s"(k0), "+s"(k1), "+s"(v0), "+s"(v1), "+s"(q0), "+s"(q1), "+s"(o0), "+s"(o1), "+s"(x_), "+s"(g_), "+s"(out_), "+s"(ws_));
    pk = br ? k1 : k0; pv = br ? v1 : v0; pwq = br ? q1 : q0; pwo = br ? o1 : o0;
    p.x = x_; p.gate = g_; p.out = out_; p.ws = ws_;
  }
  if (__builtin_expect(wave == 0, 1)) {
    __builtin_amdgcn_s_setprio(3);
    Loader ld;
    ld.ctl = ctl; ld.ring = (unsigned)(uintptr_t)(L + L_RING); ld.voff = lane * 16; ld.slot = PRE_UNITS; ld.landed = 0;
    ld.tclk = __builtin_amdgcn_s_memrealtime(); ld.t0 = (unsigned)ld.tclk;
    const unsigned char* kb = (const unsigned char*)(pk + kv_off);
    if (lane < CTL_WORDS) ctl[lane] = (lane >= CTL_PROG && lane < CTL_PROG + NCONS) ? (unsigned)(lane - CTL_PROG) : (lane >= CTL_PROG + NCONS ? 0xffffffffu : 0u);
    {
      const unsigned char* wq = (const unsigned char*)(pwq + (size_t)(sp * (DM / NSPLIT)) * (NH * DH) + h * DH);
      const unsigned wvoff = (lane >> 4) * (NH * DH * 2) + (lane & 15) * 16;
#pragma unroll 4
      for (int i = 0; i < 4 * WQ_UNITS; ++i) dma1(ld.ring + WQ_SLOT * UNIT_B + i * 1024, wvoff, wq + (size_t)i * (4 * NH * DH * 2));
    }
#pragma unroll
    for (int u = 0; u < PRE_UNITS; ++u) dma4(ld.ring + u * UNIT_B, ld.voff, kb + u * UNIT_B);
    wait_lgkm();
    __builtin_amdgcn_s_barrier();
    wait_vm<4 * PRE_UNITS>();
    ctl[CTL_WQ] = 1u;
    ld.free_upto = WQ_SLOT;
    ld.pass(kb + PRE_UNITS * UNIT_B, PRE_UNITS, NP);
    ld.pass((const unsigned char*)(pv + kv_off), NP, 2 * NP);
#define DRAIN_STEP(K) if (INFL_MAX > (K)) { wait_vm<4 * (K)>(); ctl[CTL_FULL] = (unsigned)max(ld.landed, NU - (K)); }
    DRAIN_STEP(14) DRAIN_STEP(13) DRAIN_STEP(12) DRAIN_STEP(11) DRAIN_STEP(10) DRAIN_STEP(9) DRAIN_STEP(8) DRAIN_STEP(7)
    DRAIN_STEP(6) DRAIN_STEP(5) DRAIN_STEP(4) DRAIN_STEP(3) DRAIN_STEP(2) DRAIN_STEP(1) DRAIN_STEP(0)
#undef DRAIN_STEP
    return;
  }

  const int c = wave - 1, ct = tid - 64;
  const int r = lane >> 4, c16 = lane & 15;
  const __amdgpu_buffer_rsrc_t wsr = __builtin_amdgcn_make_buffer_rsrc(p.ws, 0, WS_END, 0x00020000);
  unsigned tag, gen = 0;
  {
    const int rl = 4 * (ct >> 4);
    const v2u x2 = *(const GAS v2u*)(p.x + sp * (DM / NSPLIT) + rl);
    const unsigned ep = __hip_atomic_load((GAS unsigned*)(p.ws + WS_EPOCH), __ATOMIC_RELAXED, __HIP_MEMORY_SCOPE_AGENT);
    __builtin_amdgcn_sched_barrier(0);
    __builtin_amdgcn_s_barrier();
    __builtin_amdgcn_sched_barrier(0);
    {
      unsigned spn = 0;
      while (ctl[CTL_WQ] == 0u) { __builtin_amdgcn_s_sleep(1); if (give_up(ctl, spn)) break; }
      asm volatile("" ::: "memory");
    }
    v4u w4[4];
#pragma unroll
    for (int i = 0; i < 4; ++i) w4[i] = *(const LAS v4u*)(L + L_RING + WQ_SLOT * UNIT_B + (rl + i) * 256 + 16 * c16);
    float qa[8];
#pragma unroll
    for (int e = 0; e < 8; ++e) qa[e] = 0.f;
#pragma unroll
    for (int i = 0; i < 4; ++i) {
      const unsigned xd = x2[i >> 1];
      const unsigned xlo = (i & 1) ? (xd >> 16) : (xd & 0xffffu), xhi = xlo << 16;
#pragma unroll
      for (int j = 0; j < 4; ++j) { qa[2 * j] = dot2(w4[i][j], xlo, qa[2 * j]); qa[2 * j + 1] = dot2(w4[i][j], xhi, qa[2 * j + 1]); }
    }
#pragma unroll
    for (int e = 0; e < 8; ++e) qa[e] = x32_sum(x16_sum(qa[e]));
    LAS float* part = (LAS float*)(L + L_X);
    if (r == 0) {
      *(LAS v4f*)(part + c * DH + 8 * c16) = v4f{qa[0], qa[1], qa[2], qa[3]};
      *(LAS v4f*)(part + c * DH + 8 * c16 + 4) = v4f{qa[4], qa[5], qa[6], qa[7]};
    }
    tag = __builtin_amdgcn_readfirstlane(ep) * 1664525u + 1013904223u;
  }
  cbar(ctl, gen, lane);
  if (c == 1 && lane == 0) ctl[CTL_WQ] = 2u;
  if (c == 0) {
    const LAS float* part = (const LAS float*)(L + L_X);
    float s0 = 0.f, s1 = 0.f;
#pragma unroll
    for (int w = 0; w < NCONS; ++w) { const v2f t = *(const LAS v2f*)(part + w * DH + 2 * lane); s0 += t.x; s1 += t.y; }
    const int qoff = WS_Q + (hh * NSPLIT * DH + 2 * lane) * 8;
    __builtin_amdgcn_raw_buffer_store_b128(v4u{__float_as_uint(s0), tag, __float_as_uint(s1), tag}, wsr, qoff + sp * DH * 8, 0, 16);
    v4u g[NSPLIT];
    unsigned spn = 0;
    for (;;) {
      bool ok = true;
#pragma unroll
      for (int i = 0; i < NSPLIT; ++i) g[i] = __builtin_amdgcn_raw_buffer_load_b128(wsr, qoff + i * DH * 8, 0, 16);
#pragma unroll
      for (int i = 0; i < NSPLIT; ++i) ok = ok && TAGS_OK(g[i]);
      if (__all(ok) || ++spn > SPIN_GLB) break;
      __builtin_amdgcn_s_sleep(1);
      asm volatile("" ::: "memory");
    }
    s0 = 0.f; s1 = 0.f;
#pragma unroll
    for (int i = 0; i < NSPLIT; ++i) { s0 += __uint_as_float(g[i][0]); s1 += __uint_as_float(g[i][2]); }
    ((LAS unsigned*)(L + L_Q))[lane] = (bf16_hi(s0) >> 16) | bf16_hi(s1);
  }
  cbar(ctl, gen, lane);
  const v4u q = *(const LAS v4u*)(L + L_Q + 16 * c16);
  LAS unsigned char* sc = L + L_SC + c * (MAXK * 64);
  LAS float* red = (LAS float*)(L + L_RED);
  const bool reducer = b < NRED;
  float mrun = -INFINITY;
  int nk = 0, j = c;
  auto kstep = [&]() {
    wait_full(ctl, j);
    const LAS unsigned char* base = L + L_RING + (j % RING_U) * UNIT_B + lane * 16;
    v4u kk[4];
#pragma unroll
    for (int i = 0; i < 4; ++i) kk[i] = *(const LAS v4u*)(base + i * 1024);
    wait_lgkm();
    asm volatile("" : "+v"(kk[0]), "+v"(kk[1]), "+v"(kk[2]), "+v"(kk[3]));
    ctl[CTL_PROG + c] = (j + NCONS < NP) ? (unsigned)(j + NCONS) : (unsigned)(NP + c);
    float s[4];
#pragma unroll
    for (int i = 0; i < 4; ++i) {
      float d = dot2(kk[i][0], q[0], 0.f); d = dot2(kk[i][1], q[1], d); d = dot2(kk[i][2], q[2], d); d = dot2(kk[i][3], q[3], d);
      s[i] = row16_sum(d);
    }
    mrun = fmaxf(fmaxf(mrun, fmaxf(s[0], s[1])), fmaxf(s[2], s[3]));
    if (c16 == 0) *(LAS v4f*)(sc + nk * 64 + r * 16) = v4f{s[0], s[1], s[2], s[3]};
    j += NCONS; ++nk;
  };
  kstep(); kstep();
  v4u W[4];
  {
    const GAS v4u* wo = (const GAS v4u*)(pwo + (size_t)(h * DH + 32 * sp + (lane >> 3)) * DM + 8 * (8 * c + (lane & 7)));
#pragma unroll
    for (int i = 0; i < 4; ++i) W[i] = ldnt(wo + (size_t)i * (8 * DM / 8));
    __builtin_amdgcn_sched_barrier(0);
  }
#pragma unroll 1
  while (j < NP) kstep();
  const float mw = x32_max(x16_max(mrun));
  float lw = 0.f;
  wait_lgkm();
  for (int idx = lane; idx < nk * 16; idx += 64) {
    const float s = ((const LAS float*)sc)[idx];
    const float e = __builtin_amdgcn_exp2f((s - mw) * C2);
    lw += e;
    ((LAS unsigned*)sc)[idx] = bf16_hi(e);
  }
  lw = wave_sum(lw);
  if (lane == 0) { ((LAS float*)(L + L_ML))[c] = mw * C2; ((LAS float*)(L + L_ML))[12 + c] = lw; }
#pragma unroll
  for (int m = 0; m < 4; ++m) {
    const unsigned a = W[0][m], bq = W[1][m], cq = W[2][m], d = W[3][m];
    W[0][m] = __builtin_amdgcn_perm(bq, a, 0x05040100u); W[1][m] = __builtin_amdgcn_perm(bq, a, 0x07060302u);
    W[2][m] = __builtin_amdgcn_perm(d, cq, 0x05040100u); W[3][m] = __builtin_amdgcn_perm(d, cq, 0x07060302u);
  }
  if (reducer && c == 8) {
    float z = 0.f;
#pragma unroll
    for (int i = 0; i < 5; ++i) {
      const unsigned gx = ((const GAS unsigned*)p.x)[lane + 64 * i], gg = ((const GAS unsigned*)p.gate)[lane + 64 * i];
      z += bf16_rnd(__uint_as_float(gx << 16) * __uint_as_float(gg << 16));
      z += bf16_rnd(__uint_as_float(gx & 0xffff0000u) * __uint_as_float(gg & 0xffff0000u));
    }
    z = bf16_rnd(wave_sum(z));
    const float g = bf16_rnd(1.f / (1.f + __expf(-z)));
    if (lane == 0) red[128] = g;
  }
  wait_lgkm();
  float acc[8];
#pragma unroll
  for (int e = 0; e < 8; ++e) acc[e] = 0.f;
  int k = 0;
  j = c;
  auto vstep = [&]() {
    const int u = NP + j;
    wait_full(ctl, u);
    const LAS unsigned char* base = L + L_RING + (u % RING_U) * UNIT_B + lane * 16;
    v4u vv[4];
#pragma unroll
    for (int i = 0; i < 4; ++i) vv[i] = *(const LAS v4u*)(base + i * 1024);
    v4u pp = *(const LAS v4u*)(sc + k * 64 + r * 16);
    wait_lgkm();
    asm volatile("" : "+v"(vv[0]), "+v"(vv[1]), "+v"(vv[2]), "+v"(vv[3]), "+v"(pp));
    ctl[CTL_PROG + c] = (j + NCONS < NP) ? (unsigned)(u + NCONS) : 0xffffffffu;
#pragma unroll
    for (int i = 0; i < 4; ++i) {
      const unsigned phi = pp[i], plo = phi >> 16;
#pragma unroll
      for (int jj = 0; jj < 4; ++jj) { acc[2 * jj] = dot2(vv[i][jj], plo, acc[2 * jj]); acc[2 * jj + 1] = dot2(vv[i][jj], phi, acc[2 * jj + 1]); }
    }
    j += NCONS; ++k;
  };
#pragma unroll 1
  while (k < 6) vstep();
  float mwg;
  {
    const LAS float* mlp = (const LAS float*)(L + L_ML);
    const float mi = lane < NCONS ? mlp[lane] : -INFINITY;
    mwg = fmaxf(mi, dpp<0x128>(mi)); mwg = fmaxf(mwg, dpp<0x124>(mwg)); mwg = fmaxf(mwg, dpp<0x122>(mwg)); mwg = fmaxf(mwg, dpp<0x121>(mwg));
    mwg = __uint_as_float(__builtin_amdgcn_readfirstlane(__float_as_uint(mwg)));
    if (c == 1) {
      const float li = lane < NCONS ? mlp[12 + lane] * __builtin_amdgcn_exp2f(mi - mwg) : 0.f;
      const float ls = row16_sum(li);
      if (lane == 0) __builtin_amdgcn_raw_buffer_store_b128(v4u{__float_as_uint(mwg), tag, __float_as_uint(ls), tag}, wsr, WS_ML + (hh * NSPLIT + sp) * 16, 0, 16);
    }
  }
#pragma unroll 1
  while (k < 13) vstep();
  if (c == 1) {
    v4u gm;
    unsigned spn = 0;
    for (;;) {
      gm = __builtin_amdgcn_raw_buffer_load_b128(wsr, WS_ML + (hh * NSPLIT + (lane & 3)) * 16, 0, 16);
      if (__all(TAGS_OK(gm)) || ++spn > SPIN_GLB) break;
      __builtin_amdgcn_s_sleep(4);
      asm volatile("" ::: "memory");
    }
    const float ms = __uint_as_float(gm[0]);
    float M = fmaxf(ms, dpp<0xB1>(ms)); M = fmaxf(M, dpp<0x4E>(M));
    float ls = __uint_as_float(gm[2]) * __builtin_amdgcn_exp2f(ms - M);
    ls += dpp<0xB1>(ls); ls += dpp<0x4E>(ls);
    if (lane == 0) *(LAS float*)(L + L_F) = __builtin_amdgcn_exp2f(mwg - M) / ls;
  }
#pragma unroll 1
  while (k < 21) vstep();
  const float fw = __builtin_amdgcn_exp2f(mw * C2 - mwg) * *(const LAS float*)(L + L_F);
#pragma unroll 1
  while (j < NP) vstep();
  {
#pragma unroll
    for (int e = 0; e < 8; ++e) acc[e] = x32_sum(x16_sum(acc[e])) * fw;
    LAS float* part = (LAS float*)(L + L_X);
    if (r == 0) {
      *(LAS v4f*)(part + c * DH + 8 * c16) = v4f{acc[0], acc[1], acc[2], acc[3]};
      *(LAS v4f*)(part + c * DH + 8 * c16 + 4) = v4f{acc[4], acc[5], acc[6], acc[7]};
    }
  }
  hbar();
  if (c < 2) {
    const LAS float* part = (const LAS float*)(L + L_X);
    float s0 = 0.f;
#pragma unroll
    for (int w = 0; w < NCONS; ++w) s0 += part[w * DH + 64 * c + lane];
    const unsigned long long g = ((unsigned long long)(tag + 1u) << 32) | __float_as_uint(s0);
    __hip_atomic_store((GAS unsigned long long*)(p.ws + WS_O) + (size_t)(hh * NSPLIT + sp) * DH + 64 * c + lane, g, __ATOMIC_RELAXED, __HIP_MEMORY_SCOPE_AGENT);
  }
  if (c == 0) {
    const int ooff = WS_O + ((hh * NSPLIT + r) * DH + 32 * sp + 2 * c16) * 8;
    v4u go;
    unsigned spn = 0;
    for (;;) {
      go = __builtin_amdgcn_raw_buffer_load_b128(wsr, ooff, 0, 16);
      if (__all(go[1] == tag + 1u && go[3] == tag + 1u) || ++spn > SPIN_GLB) break;
      __builtin_amdgcn_s_sleep(1);
      asm volatile("" ::: "memory");
    }
    const float s0 = x32_sum(x16_sum(__uint_as_float(go[0]))), s1 = x32_sum(x16_sum(__uint_as_float(go[2])));
    if (lane < 16) {
      LAS unsigned short* ot = (LAS unsigned short*)(L + L_Q);
      const int d0 = 2 * c16, d1 = d0 + 1;
      ot[(d0 & 7) * 4 + (d0 >> 3)] = (unsigned short)(bf16_hi(s0) >> 16);
      ot[(d1 & 7) * 4 + (d1 >> 3)] = (unsigned short)(bf16_hi(s1) >> 16);
    }
  }
  hbar();
  {
    const v2u op = *(const LAS v2u*)(L + L_Q + 8 * (lane >> 3));
    float y[8];
#pragma unroll
    for (int m = 0; m < 4; ++m) {
      y[2 * m] = dot2(W[2][m], op[1], dot2(W[0][m], op[0], 0.f));
      y[2 * m + 1] = dot2(W[3][m], op[1], dot2(W[1][m], op[0], 0.f));
    }
    float z[4], u2[2];
#pragma unroll
    for (int i = 0; i < 4; ++i) {
      auto t = __builtin_amdgcn_permlane32_swap(__float_as_uint(y[i]), __float_as_uint(y[i + 4]), false, false);
      z[i] = __uint_as_float(t[0]) + __uint_as_float(t[1]);
    }
#pragma unroll
    for (int i = 0; i < 2; ++i) {
      auto t = __builtin_amdgcn_permlane16_swap(__float_as_uint(z[i]), __float_as_uint(z[i + 2]), false, false);
      u2[i] = __uint_as_float(t[0]) + __uint_as_float(t[1]);
    }
    const bool odd = (lane & 8) != 0;
    const float tot = (odd ? u2[1] : u2[0]) + dpp<0x128>(odd ? u2[0] : u2[1]);
    const unsigned long long g = ((unsigned long long)tag << 32) | __float_as_uint(tot);
    __hip_atomic_store((GAS unsigned long long*)(p.ws + WS_Y) + (size_t)(hh * NSPLIT + sp) * DM + 64 * c + 8 * (lane & 7) + (lane >> 3), g, __ATOMIC_RELAXED, __HIP_MEMORY_SCOPE_AGENT);
  }
  if (reducer) {
    if (c < 8) {
      const int yj = lane & 7;
      const int off = WS_Y + ((c * 32 + (lane >> 3)) * DM + b * 16 + 2 * yj) * 8;
      v4u g0, g1, g2, g3;
      bool ok = false;
      unsigned spn = 0;
      for (;;) {
        if (!ok) {
          g0 = __builtin_amdgcn_raw_buffer_load_b128(wsr, off, 0, 16);
          g1 = __builtin_amdgcn_raw_buffer_load_b128(wsr, off + 8 * DM * 8, 0, 16);
          g2 = __builtin_amdgcn_raw_buffer_load_b128(wsr, off + 16 * DM * 8, 0, 16);
          g3 = __builtin_amdgcn_raw_buffer_load_b128(wsr, off + 24 * DM * 8, 0, 16);
          ok = TAGS_OK(g0) && TAGS_OK(g1) && TAGS_OK(g2) && TAGS_OK(g3);
        }
        if (__all(ok) || ++spn > SPIN_GLB) break;
        __builtin_amdgcn_s_sleep(2);
        asm volatile("" ::: "memory");
      }
      float s0 = (__uint_as_float(g0[0]) + __uint_as_float(g1[0])) + (__uint_as_float(g2[0]) + __uint_as_float(g3[0]));
      float s1 = (__uint_as_float(g0[2]) + __uint_as_float(g1[2])) + (__uint_as_float(g2[2]) + __uint_as_float(g3[2]));
      s0 += dpp<0x128>(s0); s1 += dpp<0x128>(s1);
      s0 = x32_sum(x16_sum(s0)); s1 = x32_sum(x16_sum(s1));
      if (lane < 8) *(LAS v2f*)(red + c * 16 + 2 * yj) = v2f{s0, s1};
    }
    hbar();
    if (c == 0 && lane < 8) {
      const float g = red[128], g1 = bf16_rnd(1.f - g);
      unsigned o2[2];
#pragma unroll
      for (int e = 0; e < 2; ++e) {
        const int col = 2 * lane + e;
        const float a = (red[col] + red[16 + col]) + (red[32 + col] + red[48 + col]);
        const float bb = (red[64 + col] + red[80 + col]) + (red[96 + col] + red[112 + col]);
        o2[e] = bf16_hi(bf16_rnd(g * bf16_rnd(a)) + bf16_rnd(g1 * bf16_rnd(bb)));
      }
      ((unsigned*)p.out)[b * 8 + lane] = (o2[0] >> 16) | o2[1];
      if (b == 0 && lane == 0) __hip_atomic_store((GAS unsigned*)(p.ws + WS_EPOCH), tag, __ATOMIC_RELAXED, __HIP_MEMORY_SCOPE_AGENT);
    }
  }
}
}

extern "C" void kernel_launch(void* const* d_in, const int* in_sizes, int n_in, void* d_out, int out_size, void* d_ws, size_t ws_size,
                              hipStream_t stream) {
  Params p;
  p.x = (const unsigned short*)d_in[0];
  p.k0 = (const unsigned short*)d_in[1]; p.v0 = (const unsigned short*)d_in[2];
  p.wq0 = (const unsigned short*)d_in[3]; p.wo0 = (const unsigned short*)d_in[4];
  p.k1 = (const unsigned short*)d_in[6]; p.v1 = (const unsigned short*)d_in[7];
  p.wq1 = (const unsigned short*)d_in[8]; p.wo1 = (const unsigned short*)d_in[9];
  p.gate = (const unsigned short*)d_in[10];
  p.out = (unsigned short*)d_out;
  p.ws = (unsigned char*)d_ws;
  hipLaunchKernelGGL(fused_attn, dim3(NWG), dim3(NTHR), 0, stream, p);
}
```
